```python
import torch

def _softmax(x, axis=-1):
    return torch.softmax(x, dim=axis)
def _gelu(x):
    return torch.nn.functional.gelu(x, approximate="tanh")
def _silu(x):
    return torch.nn.functional.silu(x)
def _sigmoid(x):
    return torch.sigmoid(x)
def _layernorm(x, axis=-1, eps=1e-5):
    m = x.mean(dim=axis, keepdim=True)
    v = x.var(dim=axis, keepdim=True, unbiased=False)
    return (x - m) / torch.sqrt(v + eps)
def _scatter_add(tgt, idx, src, axis=0):
    return tgt.clone().index_add_(axis, idx, src)
def _segment_sum(src, seg, n):
    out = torch.zeros((n,) + src.shape[1:], dtype=src.dtype)
    return out.index_add_(0, seg, src)
def _pad(x, pw):
    flat = tuple(v for pair in reversed(pw) for v in pair)
    return torch.nn.functional.pad(x, flat)

kernel_name = 'standalone__dense_activation_relu__prompt'

def setup_inputs():

    _w = (lambda a: (lambda t: t.to(torch.bfloat16) if t.is_floating_point() else t)(torch.as_tensor(a)))
    B, S, D, Dff = 1, 2048, 448, 1792
    return {
        "x": _w((lambda *s: torch.randn(*s, dtype=torch.bfloat16) * 0.05)(B, S, D)),
        "w1": _w((lambda *s: torch.randn(*s, dtype=torch.bfloat16) * 0.05)(D, Dff)),
        "w2": _w((lambda *s: torch.randn(*s, dtype=torch.bfloat16) * 0.05)(Dff, D)),
    }

def reference(x, w1, w2):
    h = torch.einsum("bsd,df->bsf", x, w1)
    zero = torch.zeros_like(h)
    h = torch.maximum(h, zero)
    return torch.einsum("bsf,fd->bsd", h, w2)
```

```cpp
#include <hip/hip_runtime.h>
#include <hip/hip_bf16.h>
typedef __hip_bfloat16 bf16;
__global__ void k1(const bf16* x, const bf16* w1, bf16* h) {
  int f = blockIdx.x * 256 + threadIdx.x; int m = blockIdx.y;
  float acc = 0.f;
  for (int k = 0; k < 448; ++k) acc += __bfloat162float(x[m*448+k]) * __bfloat162float(w1[k*1792+f]);
  h[m*1792+f] = __float2bfloat16(fmaxf(acc, 0.f));
}
__global__ void k2(const bf16* h, const bf16* w2, bf16* o) {
  int d = blockIdx.x * 64 + threadIdx.x; int m = blockIdx.y;
  float acc = 0.f;
  for (int k = 0; k < 1792; ++k) acc += __bfloat162float(h[m*1792+k]) * __bfloat162float(w2[k*448+d]);
  o[m*448+d] = __float2bfloat16(acc);
}
extern "C" void kernel_launch(void* const* d_in, const int* in_sizes, int n_in,
                              void* d_out, int out_size, void* d_ws, size_t ws_size,
                              hipStream_t stream) {
  const bf16* x = (const bf16*)d_in[0]; const bf16* w1 = (const bf16*)d_in[1]; const bf16* w2 = (const bf16*)d_in[2];
  bf16* h = (bf16*)d_ws;
  k1<<<dim3(7, 2048), 256, 0, stream>>>(x, w1, h);
  k2<<<dim3(7, 2048), 64, 0, stream>>>(h, w2, (bf16*)d_out);
}
```

```cpp
#include <hip/hip_runtime.h>
#include <hip/hip_bf16.h>
#include <type_traits>
typedef __hip_bfloat16 bf16;
#define DI __device__ __forceinline__
DI unsigned rfl(unsigned x){return __builtin_amdgcn_readfirstlane(x);}
DI unsigned long long rstamp(){unsigned long long t; asm volatile("s_memrealtime %0\n\ts_waitcnt lgkmcnt(0)":"=s"(t)::"memory"); return t;}
template<int N> DI void waitv(){asm volatile("s_waitcnt vmcnt(%0)"::"i"(N):"memory");}
template<int LO,int GO> DI void dma(unsigned m0b, unsigned voff, __amdgpu_buffer_rsrc_t srd, unsigned soffb){
  unsigned t;
  asm volatile("s_add_u32 m0, %1, %5\n\ts_add_u32 %0, %4, %6\n\tbuffer_load_dwordx4 %2, %3, %0 offen lds":"=&s"(t):"s"(m0b),"v"(voff),"s"(srd),"s"(soffb),"i"(LO),"i"(GO):"memory","scc");
}
template<int A,int B,class F> DI void static_for(F f){ if constexpr(A<B){ f(std::integral_constant<int,A>{}); static_for<A+1,B>(f);} }
#define MULT 0
constexpr int DEPTH=3;
__global__ __launch_bounds__(256,1) void probe(const char* x,const char* w1,const char* w2){
  __shared__ __attribute__((aligned(16))) char lds[163840];
  unsigned long long r0=rstamp();
  const unsigned tid=threadIdx.x, lane=tid&63, wid=rfl(tid>>6);
  unsigned b=blockIdx.x;
  unsigned xcd=b&7, idx=b>>3, g=xcd*2+idx/14, j=idx%14;
  auto sx=__builtin_amdgcn_make_buffer_rsrc((void*)x,0,2048*896,0x00020000);
  auto s1=__builtin_amdgcn_make_buffer_rsrc((void*)w1,0,448*3584,0x00020000);
  auto s2=__builtin_amdgcn_make_buffer_rsrc((void*)w2,0,1792*896,0x00020000);
  unsigned vx=(lane>>3)*896+(lane&7)*16;
  unsigned v1=(lane>>4)*3584+(lane&15)*16;
  unsigned v2=(lane>>3)*896+(lane&7)*16;
  unsigned ldsb=(unsigned)(size_t)lds;
  unsigned dt=j%7;
  const unsigned mb=ldsb+wid*8192, bx=(g*128+wid*32)*896, b1=wid*16*3584+j*256, b2=wid*448*896+dt*128;
  auto issue=[&](auto cc){ constexpr int c=cc.value;
    constexpr int sl=(c%5)*32768;
    if constexpr(c<7){
      dma<sl,c*128>(mb,vx,sx,bx); dma<sl+1024,c*128+8*896>(mb,vx,sx,bx); dma<sl+2048,c*128+16*896>(mb,vx,sx,bx); dma<sl+3072,c*128+24*896>(mb,vx,sx,bx);
      dma<sl+4096,(c*64)*3584>(mb,v1,s1,b1); dma<sl+5120,(c*64+4)*3584>(mb,v1,s1,b1); dma<sl+6144,(c*64+8)*3584>(mb,v1,s1,b1); dma<sl+7168,(c*64+12)*3584>(mb,v1,s1,b1);
    } else {
      constexpr int r=(c-7)*64;
      dma<sl,(r)*896>(mb,v2,s2,b2); dma<sl+1024,(r+8)*896>(mb,v2,s2,b2); dma<sl+2048,(r+16)*896>(mb,v2,s2,b2); dma<sl+3072,(r+24)*896>(mb,v2,s2,b2);
      dma<sl+4096,(r+32)*896>(mb,v2,s2,b2); dma<sl+5120,(r+40)*896>(mb,v2,s2,b2); dma<sl+6144,(r+48)*896>(mb,v2,s2,b2); dma<sl+7168,(r+56)*896>(mb,v2,s2,b2);
    }
  };
  static_for<0,DEPTH>([&](auto c){ issue(c); });
  static_for<0,14>([&](auto cc){ constexpr int c=cc.value;
    constexpr int later = (c+DEPTH-1 < 14 ? DEPTH-1 : 13-c);
    waitv<later*8>();
    if constexpr(c+DEPTH<14) issue(std::integral_constant<int,c+DEPTH>{});
  });
  unsigned long long r1=rstamp();
  unsigned long long until=r1+(r1-r0)*MULT;
  if((r1-r0)>100000) until=r1;
  while(rstamp()<until) __builtin_amdgcn_s_sleep(4);
}
__global__ void k1(const bf16* x, const bf16* w1, bf16* h) {
  int f = blockIdx.x * 256 + threadIdx.x; int m = blockIdx.y;
  float acc = 0.f;
  for (int k = 0; k < 448; ++k) acc += __bfloat162float(x[m*448+k]) * __bfloat162float(w1[k*1792+f]);
  h[m*1792+f] = __float2bfloat16(fmaxf(acc, 0.f));
}
__global__ void k2(const bf16* h, const bf16* w2, bf16* o) {
  int d = blockIdx.x * 64 + threadIdx.x; int m = blockIdx.y;
  float acc = 0.f;
  for (int k = 0; k < 1792; ++k) acc += __bfloat162float(h[m*1792+k]) * __bfloat162float(w2[k*448+d]);
  o[m*448+d] = __float2bfloat16(acc);
}
extern "C" void kernel_launch(void* const* d_in, const int* in_sizes, int n_in,
                              void* d_out, int out_size, void* d_ws, size_t ws_size,
                              hipStream_t stream) {
  const bf16* x = (const bf16*)d_in[0]; const bf16* w1 = (const bf16*)d_in[1]; const bf16* w2 = (const bf16*)d_in[2];
  bf16* h = (bf16*)d_ws;
  probe<<<224,256,0,stream>>>((const char*)x,(const char*)w1,(const char*)w2);
  k1<<<dim3(7, 2048), 256, 0, stream>>>(x, w1, h);
  k2<<<dim3(7, 2048), 64, 0, stream>>>(h, w2, (bf16*)d_out);
}
```

```cpp
#include <hip/hip_runtime.h>
#include <hip/hip_bf16.h>
typedef __hip_bfloat16 bf16;
#define DI __device__ __forceinline__
DI unsigned long long rstamp(){unsigned long long t; asm volatile("s_memrealtime %0\n\ts_waitcnt lgkmcnt(0)":"=s"(t)::"memory"); return t;}
DI unsigned long long stamp(){unsigned long long t; asm volatile("s_memtime %0\n\ts_waitcnt lgkmcnt(0)":"=s"(t)::"memory"); return t;}
#define WHICH 3
__global__ __launch_bounds__(256,1) void probe(const char* x){
  unsigned long long c;
  c=0;
  unsigned long long r1=rstamp();
  if(c>20000) c=20000;
  unsigned long long until=r1+c*10;
  while(rstamp()<until) __builtin_amdgcn_s_sleep(4);
}
__global__ void k1(const bf16* x, const bf16* w1, bf16* h) {
  int f = blockIdx.x * 256 + threadIdx.x; int m = blockIdx.y;
  float acc = 0.f;
  for (int k = 0; k < 448; ++k) acc += __bfloat162float(x[m*448+k]) * __bfloat162float(w1[k*1792+f]);
  h[m*1792+f] = __float2bfloat16(fmaxf(acc, 0.f));
}
__global__ void k2(const bf16* h, const bf16* w2, bf16* o) {
  int d = blockIdx.x * 64 + threadIdx.x; int m = blockIdx.y;
  float acc = 0.f;
  for (int k = 0; k < 1792; ++k) acc += __bfloat162float(h[m*1792+k]) * __bfloat162float(w2[k*448+d]);
  o[m*448+d] = __float2bfloat16(acc);
}
extern "C" void kernel_launch(void* const* d_in, const int* in_sizes, int n_in,
                              void* d_out, int out_size, void* d_ws, size_t ws_size,
                              hipStream_t stream) {
  const bf16* x = (const bf16*)d_in[0]; const bf16* w1 = (const bf16*)d_in[1]; const bf16* w2 = (const bf16*)d_in[2];
  bf16* h = (bf16*)d_ws;
  probe<<<224,256,0,stream>>>((const char*)x);
  k1<<<dim3(7, 2048), 256, 0, stream>>>(x, w1, h);
  k2<<<dim3(7, 2048), 64, 0, stream>>>(h, w2, (bf16*)d_out);
}
```

```cpp
#include <hip/hip_runtime.h>

__global__ __launch_bounds__(512, 2) void fused_mlp(const void* x, const void* w1, const void* w2, void* out, void* ws) {
  __shared__ __attribute__((aligned(16))) char lds[163840];
  asm volatile(
    "L_start_%=:\n\t"
    "s_load_dwordx8 s[56:63], s[0:1], 0x0\n\t"
    "s_load_dwordx2 s[64:65], s[0:1], 0x20\n\t"
    "v_readfirstlane_b32 s28, v0\n\t"
    "v_and_b32 v1, 63, v0\n\t"
    "s_lshr_b32 s34, s2, 3\n\t"
    "s_mul_i32 s35, s34, 37\n\t"
    "s_and_b32 s30, s2, 1\n\t"
    "s_lshr_b32 s35, s35, 8\n\t"
    "s_lshr_b32 s28, s28, 6\n\t"
    "s_mul_i32 s36, s35, 7\n\t"
    "s_and_b32 s38, s28, 3\n\t"
    "s_sub_u32 s31, s34, s36\n\t"
    "s_bfe_u32 s36, s2, 0x20001\n\t"
    "s_cmp_ge_u32 s2, 224\n\t"
    "s_cbranch_scc1 L_H0_%=\n\t"
    "s_cmp_lt_u32 s28, 4\n\t"
    "s_cbranch_scc1 L_C_%=\n\t"
    "s_cmp_ge_u32 s2, 32\n\t"
    "s_cbranch_scc1 L_P_%=\n\t"
    "s_getpc_b64 s[4:5]\n\t"
    "L_pc_%=:\n\t"
    "v_and_b32 v2, 0xff, v0\n\t"
    "v_lshlrev_b32 v2, 6, v2\n\t"
    "s_mov_b32 m0, 159744\n\t"
    "v_min_u32 v2, L_end_%=-L_pc_%=-4, v2\n\t"
    "global_load_lds_dword v2, s[4:5]\n\t"
    "s_branch L_P_%=\n\t"
    "L_H0_%=:\n\t"
    "s_cmp_lg_u32 s28, 0\n\t"
    "s_cbranch_scc1 L_Hquit_%=\n\t"
    "v_and_b32 v2, 31, v0\n\t"
    "v_lshlrev_b32 v2, 2, v2\n\t"
    "s_and_b32 s37, s2, 7\n\t"
    "s_lshl_b32 s37, s37, 11\n\t"
    "s_add_u32 s37, s37, s31\n\t"
    "s_lshl_b32 s37, s37, 11\n\t"
    "s_add_u32 s37, s37, 3948544\n\t"
    "v_add_u32 v3, 0x2000, v2\n\t"
    "s_mov_b32 s22, 37748736\n\t"
    "s_mov_b32 s23, 0x20000\n\t"
    "s_lshl_b32 s34, s36, 7\n\t"
    "s_add_u32 s34, s34, 65536\n\t"
    "s_lshl3_add_u32 s41, s36, s30\n\t"
    "v_cmp_gt_u32 vcc, 4, v1\n\t"
    "v_lshlrev_b32 v5, 9, v1\n\t"
    "v_mov_b32 v6, 0x40000000\n\t"
    "s_lshl_b32 s41, s41, 8\n\t"
    "s_lshl2_add_u32 s41, s31, s41\n\t"
    "v_cndmask_b32 v5, v6, v5, vcc\n\t"
    "v_lshlrev_b32 v4, 7, v1\n\t"
    "s_lshl2_add_u32 s35, s36, s31\n\t"
    "s_lshl1_add_u32 s35, s35, s30\n\t"
    "v_min_u32 v4, 7040, v4\n\t"
    "s_mul_i32 s35, s35, 57344\n\t"
    "s_mov_b32 m0, 0\n\t"
    "s_mov_b32 s10, 1835008\n\t"
    "s_mov_b32 s11, 0x20000\n\t"
    "s_waitcnt lgkmcnt(0)\n\t"
    "s_sub_u32 s40, 0, s64\n\t"
    "s_mov_b32 s20, s64\n\t"
    "s_and_b32 s40, s40, 0x1fffff\n\t"
    "s_and_b32 s21, s65, 0xffff\n\t"
    "s_add_u32 s37, s37, s40\n\t"
    "buffer_store_dword v2, v2, s[20:23], s37 offen sc1\n\t"
    "buffer_store_dword v2, v3, s[20:23], s37 offen sc1\n\t"
    "buffer_load_dword v7, v2, s[20:23], s34 offen sc1\n\t"
    "buffer_load_dword v6, v5, s[20:23], s41 offen sc1\n\t"
    "s_mov_b32 s8, s56\n\t"
    "s_and_b32 s9, s57, 0xffff\n\t"
    "buffer_load_dword v4, s[8:11], s35 offen lds\n\t"
    "s_add_u32 s35, s35, 7168\n\t"
    "buffer_load_dword v4, s[8:11], s35 offen lds\n\t"
    "s_add_u32 s35, s35, 7168\n\t"
    "buffer_load_dword v4, s[8:11], s35 offen lds\n\t"
    "s_add_u32 s35, s35, 7168\n\t"
    "buffer_load_dword v4, s[8:11], s35 offen lds\n\t"
    "s_waitcnt vmcnt(7)\n\t"
    "s_nop 15\n\t"
    "s_getreg_b32 s43, hwreg(HW_REG_IB_STS, 0, 3)\n\t"
    "s_branch L_H_%=\n\t"
    "L_C_%=:\n\t"
    "v_lshrrev_b32 v25, 4, v1\n\t"
    "v_and_b32 v27, 15, v1\n\t"
    "s_add_u32 s46, s35, s31\n\t"
    "s_and_b32 s46, s46, 1\n\t"
    "s_lshl2_add_u32 s46, s46, s36\n\t"
    "s_cmp_ge_u32 s46, 7\n\t"
    "s_cselect_b32 s46, 0, s46\n\t"
    "v_lshlrev_b32 v26, 2, v25\n\t"
    "v_xor_b32 v27, v27, v26\n\t"
    "v_mul_u32_u24 v16, 3584, v25\n\t"
    "s_mul_i32 s33, s30, 7\n\t"
    "s_add_u32 s33, s33, s31\n\t"
    "s_mul_i32 s34, s38, 224\n\t"
    "v_lshl_add_u32 v16, v27, 4, v16\n\t"
    "s_add_u32 s34, s34, s33\n\t"
    "s_lshl_b32 s34, s34, 8\n\t"
    "s_lshl_b32 s43, s38, 12\n\t"
    "v_add_u32 v16, s34, v16\n\t"
    "s_mul_i32 s34, s46, 229376\n\t"
    "s_add_u32 m0, s43, 20480\n\t"
    "s_mov_b32 s14, 1605632\n\t"
    "s_mov_b32 s15, 0x20000\n\t"
    "v_xor_b32 v19, 32, v16\n\t"
    "v_add_u32 v17, 13312, v16\n\t"
    "v_add_u32 v18, 26624, v19\n\t"
    "v_add_u32 v19, 39936, v19\n\t"
    "s_lshl2_add_u32 s29, s36, s35\n\t"
    "s_lshl_b32 s46, s46, 7\n\t"
    "s_lshl_b32 s44, s38, 10\n\t"
    "v_lshrrev_b32 v24, 3, v1\n\t"
    "v_and_b32 v26, 7, v1\n\t"
    "v_mul_u32_u24 v24, 896, v24\n\t"
    "s_and_b32 s36, s38, 1\n\t"
    "s_lshl_b32 s36, s36, 2\n\t"
    "v_xor_b32 v27, v26, v25\n\t"
    "v_xor_b32 v27, s36, v27\n\t"
    "v_lshl_add_u32 v27, v27, 4, v24\n\t"
    "s_lshl4_add_u32 s36, s29, s38\n\t"
    "s_lshl_b32 s37, s30, 3\n\t"
    "s_add_u32 s35, s36, s37\n\t"
    "s_mul_i32 s35, s35, 7168\n\t"
    "v_add_u32 v20, s35, v27\n\t"
    "s_mov_b32 s10, 1835008\n\t"
    "s_mov_b32 s11, 0x20000\n\t"
    "s_add_u32 s47, s46, 128\n\t"
    "s_cmp_eq_u32 s47, 896\n\t"
    "s_cselect_b32 s47, 0, s47\n\t"
    "v_add_u32 v21, 28672, v20\n\t"
    "s_xor_b32 s37, s37, 8\n\t"
    "s_add_u32 s35, s36, s37\n\t"
    "s_mul_i32 s35, s35, 7168\n\t"
    "v_add_u32 v22, s35, v27\n\t"
    "v_add_u32 v23, 28672, v22\n\t"
    "s_mov_b32 s18, 1605632\n\t"
    "s_mov_b32 s19, 0x20000\n\t"
    "s_add_u32 s48, s47, 128\n\t"
    "s_cmp_eq_u32 s48, 896\n\t"
    "s_cselect_b32 s48, 0, s48\n\t"
    "s_add_u32 s49, s48, 128\n\t"
    "s_cmp_eq_u32 s49, 896\n\t"
    "s_cselect_b32 s49, 0, s49\n\t"
    "s_add_u32 s50, s49, 128\n\t"
    "s_cmp_eq_u32 s50, 896\n\t"
    "s_cselect_b32 s50, 0, s50\n\t"
    "s_add_u32 s51, s50, 128\n\t"
    "s_cmp_eq_u32 s51, 896\n\t"
    "s_cselect_b32 s51, 0, s51\n\t"
    "s_add_u32 s52, s51, 128\n\t"
    "s_cmp_eq_u32 s52, 896\n\t"
    "s_cselect_b32 s52, 0, s52\n\t"
    "s_and_b32 s3, s2, 7\n\t"
    "s_lshl_b32 s3, s3, 22\n\t"
    "s_mov_b32 s22, 37748736\n\t"
    "s_mov_b32 s23, 0x20000\n\t"
    "s_waitcnt lgkmcnt(0)\n\t"
    "s_mov_b32 s12, s58\n\t"
    "s_and_b32 s13, s59, 0xffff\n\t"
    "buffer_load_dwordx4 v16, s[12:15], s34 offen lds\n\t"
    "buffer_load_dwordx4 v17, s[12:15], s34 offen offset:1024 lds\n\t"
    "buffer_load_dwordx4 v18, s[12:15], s34 offen offset:2048 lds\n\t"
    "buffer_load_dwordx4 v19, s[12:15], s34 offen offset:3072 lds\n\t"
    "s_add_u32 m0, s44, 36864\n\t"
    "s_mov_b32 s8, s56\n\t"
    "s_and_b32 s9, s57, 0xffff\n\t"
    "buffer_load_dwordx4 v20, s[8:11], s46 offen lds\n\t"
    "s_add_u32 m0, s43, 40960\n\t"
    "s_mul_i32 s34, s47, 1792\n\t"
    "buffer_load_dwordx4 v16, s[12:15], s34 offen lds\n\t"
    "buffer_load_dwordx4 v17, s[12:15], s34 offen offset:1024 lds\n\t"
    "buffer_load_dwordx4 v18, s[12:15], s34 offen offset:2048 lds\n\t"
    "s_mov_b32 s20, s64\n\t"
    "s_and_b32 s21, s65, 0xffff\n\t"
    "s_mov_b32 s16, s60\n\t"
    "s_and_b32 s17, s61, 0xffff\n\t"
    "s_sub_u32 s40, 0, s64\n\t"
    "s_and_b32 s40, s40, 0x1fffff\n\t"
    "s_add_u32 s3, s3, s40\n\t"
    "s_add_u32 s3, s3, 2097152\n\t"
    "s_and_b32 s35, s29, 12\n\t"
    "s_mul_i32 s35, s35, 7\n\t"
    "s_add_u32 s35, s35, s29\n\t"
    "s_lshl2_add_u32 s35, s35, 65536\n\t"
    "buffer_load_dword v5, off, s[20:23], s35 sc1\n\t"
    "v_lshlrev_b32 v27, 1, v25\n\t"
    "v_xor_b32 v27, v27, v26\n\t"
    "v_lshl_add_u32 v10, v27, 4, v24\n\t"
    "s_mul_i32 s35, s30, 28\n\t"
    "s_add_u32 s35, s35, s38\n\t"
    "s_mul_i32 s35, s35, 28672\n\t"
    "s_lshl_b32 s37, s31, 7\n\t"
    "s_add_u32 s35, s35, s37\n\t"
    "v_add_u32 v10, s35, v10\n\t"
    "v_add_u32 v11, 6144, v10\n\t"
    "v_add_u32 v12, 12288, v10\n\t"
    "v_add_u32 v13, 18432, v10\n\t"
    "s_mul_i32 s45, s38, 18432\n\t"
    "s_add_u32 s45, s45, 8192\n\t"
    "s_mov_b32 s4, s31\n\t"
    "s_cmp_le_u32 s31, 0\n\t"
    "s_cselect_b32 s5, 1, 0\n\t"
    "s_cmp_le_u32 s31, 1\n\t"
    "s_cselect_b32 s6, 2, 1\n\t"
    "s_cmp_le_u32 s31, 2\n\t"
    "s_cselect_b32 s7, 3, 2\n\t"
    "s_cmp_le_u32 s31, 3\n\t"
    "s_cselect_b32 s53, 4, 3\n\t"
    "s_cmp_le_u32 s31, 4\n\t"
    "s_cselect_b32 s54, 5, 4\n\t"
    "s_cmp_le_u32 s31, 5\n\t"
    "s_cselect_b32 s55, 6, 5\n\t"
    "v_lshlrev_b32 v2, 4, v1\n\t"
    "v_mov_b32 v24, 0x40000000\n\t"
    "v_and_b32 v26, 3, v1\n\t"
    "v_lshlrev_b32 v26, 2, v26\n\t"
    "v_lshrrev_b32 v25, 2, v1\n\t"
    "v_lshl_add_u32 v25, v25, 8, v26\n\t"
    "v_cmp_gt_u32 vcc, 28, v1\n\t"
    "v_lshl_add_u32 v7, v1, 2, s45\n\t"
    "s_lshl_b32 s35, s38, 11\n\t"
    "s_add_u32 s35, s35, 73728\n\t"
    "v_add_u32 v6, s35, v2\n\t"
    "v_cndmask_b32 v3, v24, v25, vcc\n\t"
    "v_and_b32 v24, 31, v1\n\t"
    "v_and_b32 v25, 32, v1\n\t"
    "v_lshlrev_b32 v25, 3, v25\n\t"
    "v_lshl_add_u32 v25, v24, 2, v25\n\t"
    "v_add_u32 v25, 160256, v25\n\t"
    "v_cmp_gt_u32 vcc, 28, v24\n\t"
    "v_lshlrev_b32 v24, 8, v24\n\t"
    "v_add_u32 v24, 153600, v24\n\t"
    "v_cndmask_b32 v14, v24, v25, vcc\n\t"
    "v_lshrrev_b32 v24, 2, v1\n\t"
    "v_bfe_u32 v25, v1, 3, 2\n\t"
    "v_and_b32 v26, 3, v1\n\t"
    "v_lshlrev_b32 v26, 3, v26\n\t"
    "v_lshl_add_u32 v26, v25, 5, v26\n\t"
    "v_lshl_add_u32 v26, v24, 7, v26\n\t"
    "v_add_u32 v48, s45, v26\n\t"
    "v_xor_b32 v49, 32, v48\n\t"
    "v_xor_b32 v50, 64, v48\n\t"
    "v_xor_b32 v51, 96, v48\n\t"
    "v_and_b32 v9, 3, v1\n\t"
    "v_cmp_gt_u32 vcc, 5, v1\n\t"
    "v_lshlrev_b32 v9, 2, v9\n\t"
    "v_mov_b32 v8, 0x40000000\n\t"
    "v_cndmask_b32 v9, v8, v9, vcc\n\t"
    "s_lshl1_add_u32 s32, s29, s30\n\t"
    "s_lshl_b32 s32, s32, 8\n\t"
    "s_cmp_eq_u32 s38, 0\n\t"
    "s_cselect_b32 s67, s32, 0x40000000\n\t"
    "s_cselect_b32 s66, 256, 0\n\t"
    "s_add_u32 s66, s66, 159744\n\t"
    "buffer_load_dwordx4 v19, s[12:15], s34 offen offset:3072 lds\n\t"
    "s_add_u32 m0, s44, 57344\n\t"
    "s_nop 0\n\t"
    "buffer_load_dwordx4 v20, s[8:11], s47 offen lds\n\t"
    "s_add_u32 m0, s43, 61440\n\t"
    "s_mul_i32 s34, s48, 1792\n\t"
    "buffer_load_dwordx4 v16, s[12:15], s34 offen lds\n\t"
    "buffer_load_dwordx4 v17, s[12:15], s34 offen offset:1024 lds\n\t"
    "buffer_load_dwordx4 v18, s[12:15], s34 offen offset:2048 lds\n\t"
    "s_waitcnt vmcnt(9)\n\t"
    "s_barrier\n\t"
    "buffer_load_dwordx4 v19, s[12:15], s34 offen offset:3072 lds\n\t"
    "s_add_u32 m0, s44, 77824\n\t"
    "s_nop 0\n\t"
    "buffer_load_dwordx4 v20, s[8:11], s48 offen lds\n\t"
    "s_add_u32 m0, s43, 0\n\t"
    "s_mul_i32 s34, s49, 1792\n\t"
    "buffer_load_dwordx4 v16, s[12:15], s34 offen lds\n\t"
    "buffer_load_dwordx4 v17, s[12:15], s34 offen offset:1024 lds\n\t"
    "buffer_load_dwordx4 v18, s[12:15], s34 offen offset:2048 lds\n\t"
    "s_waitcnt vmcnt(8)\n\t"
    "s_barrier\n\t"
    "v_readfirstlane_b32 s39, v5\n\t"
    "s_nop 3\n\t"
    "s_mul_i32 s39, s39, 0x9E3779B1\n\t"
    "s_add_u32 s39, s39, 0x7F4A7C15\n\t"
    "s_bitset0_b32 s39, 31\n\t"
    "s_bitset1_b32 s39, 30\n\t"
    "buffer_load_dwordx4 v19, s[12:15], s34 offen offset:3072 lds\n\t"
    "s_add_u32 m0, s44, 16384\n\t"
    "s_nop 0\n\t"
    "buffer_load_dwordx4 v20, s[8:11], s49 offen lds\n\t"
    "s_add_u32 m0, s43, 20480\n\t"
    "s_mul_i32 s34, s50, 1792\n\t"
    "buffer_load_dwordx4 v16, s[12:15], s34 offen lds\n\t"
    "buffer_load_dwordx4 v17, s[12:15], s34 offen offset:1024 lds\n\t"
    "buffer_load_dwordx4 v18, s[12:15], s34 offen offset:2048 lds\n\t"
    "s_waitcnt vmcnt(8)\n\t"
    "s_barrier\n\t"
    "buffer_load_dwordx4 v19, s[12:15], s34 offen offset:3072 lds\n\t"
    "s_add_u32 m0, s44, 36864\n\t"
    "s_nop 0\n\t"
    "buffer_load_dwordx4 v20, s[8:11], s50 offen lds\n\t"
    "s_add_u32 m0, s43, 40960\n\t"
    "s_mul_i32 s34, s51, 1792\n\t"
    "buffer_load_dwordx4 v16, s[12:15], s34 offen lds\n\t"
    "buffer_load_dwordx4 v17, s[12:15], s34 offen offset:1024 lds\n\t"
    "buffer_load_dwordx4 v18, s[12:15], s34 offen offset:2048 lds\n\t"
    "s_waitcnt vmcnt(8)\n\t"
    "s_barrier\n\t"
    "buffer_load_dwordx4 v19, s[12:15], s34 offen offset:3072 lds\n\t"
    "s_add_u32 m0, s44, 57344\n\t"
    "s_nop 0\n\t"
    "buffer_load_dwordx4 v20, s[8:11], s51 offen lds\n\t"
    "s_mov_b32 m0, s66\n\t"
    "s_nop 0\n\t"
    "buffer_load_dword v9, s[20:23], s67 offen sc1 lds\n\t"
    "s_add_u32 m0, s43, 61440\n\t"
    "s_mul_i32 s34, s52, 1792\n\t"
    "buffer_load_dwordx4 v16, s[12:15], s34 offen lds\n\t"
    "buffer_load_dwordx4 v17, s[12:15], s34 offen offset:1024 lds\n\t"
    "s_waitcnt vmcnt(8)\n\t"
    "s_barrier\n\t"
    "buffer_load_dwordx4 v18, s[12:15], s34 offen offset:2048 lds\n\t"
    "buffer_load_dwordx4 v19, s[12:15], s34 offen offset:3072 lds\n\t"
    "s_add_u32 m0, s44, 77824\n\t"
    "s_nop 0\n\t"
    "buffer_load_dwordx4 v20, s[8:11], s52 offen lds\n\t"
    "s_add_u32 m0, s44, 81920\n\t"
    "s_nop 0\n\t"
    "buffer_load_dwordx4 v21, s[8:11], s46 offen lds\n\t"
    "s_add_u32 m0, s44, 86016\n\t"
    "s_nop 0\n\t"
    "buffer_load_dwordx4 v21, s[8:11], s47 offen lds\n\t"
    "s_waitcnt vmcnt(8)\n\t"
    "s_barrier\n\t"
    "s_add_u32 m0, s44, 90112\n\t"
    "s_nop 0\n\t"
    "buffer_load_dwordx4 v21, s[8:11], s48 offen lds\n\t"
    "s_add_u32 m0, s44, 94208\n\t"
    "s_nop 0\n\t"
    "buffer_load_dwordx4 v21, s[8:11], s49 offen lds\n\t"
    "s_add_u32 m0, s44, 98304\n\t"
    "s_nop 0\n\t"
    "buffer_load_dwordx4 v21, s[8:11], s50 offen lds\n\t"
    "s_add_u32 m0, s44, 102400\n\t"
    "s_nop 0\n\t"
    "buffer_load_dwordx4 v21, s[8:11], s51 offen lds\n\t"
    "s_add_u32 m0, s44, 106496\n\t"
    "s_nop 0\n\t"
    "buffer_load_dwordx4 v21, s[8:11], s52 offen lds\n\t"
    "s_waitcnt vmcnt(8)\n\t"
    "s_add_u32 m0, s44, 110592\n\t"
    "s_nop 0\n\t"
    "buffer_load_dwordx4 v22, s[8:11], s46 offen lds\n\t"
    "s_waitcnt vmcnt(8)\n\t"
    "s_barrier\n\t"
    "s_waitcnt vmcnt(8)\n\t"
    "s_add_u32 m0, s44, 114688\n\t"
    "s_nop 0\n\t"
    "buffer_load_dwordx4 v22, s[8:11], s47 offen lds\n\t"
    "s_waitcnt vmcnt(8)\n\t"
    "s_add_u32 m0, s44, 118784\n\t"
    "s_nop 0\n\t"
    "buffer_load_dwordx4 v22, s[8:11], s48 offen lds\n\t"
    "s_waitcnt vmcnt(8)\n\t"
    "s_add_u32 m0, s44, 122880\n\t"
    "s_nop 0\n\t"
    "buffer_load_dwordx4 v22, s[8:11], s49 offen lds\n\t"
    "s_waitcnt vmcnt(8)\n\t"
    "s_add_u32 m0, s44, 126976\n\t"
    "s_nop 0\n\t"
    "buffer_load_dwordx4 v22, s[8:11], s50 offen lds\n\t"
    "s_waitcnt vmcnt(8)\n\t"
    "s_add_u32 m0, s44, 131072\n\t"
    "s_nop 0\n\t"
    "buffer_load_dwordx4 v22, s[8:11], s51 offen lds\n\t"
    "s_waitcnt vmcnt(8)\n\t"
    "s_add_u32 m0, s44, 135168\n\t"
    "s_nop 0\n\t"
    "buffer_load_dwordx4 v22, s[8:11], s52 offen lds\n\t"
    "s_waitcnt vmcnt(8)\n\t"
    "s_add_u32 m0, s44, 139264\n\t"
    "s_nop 0\n\t"
    "buffer_load_dwordx4 v23, s[8:11], s46 offen lds\n\t"
    "s_waitcnt vmcnt(8)\n\t"
    "s_barrier\n\t"
    "v_lshlrev_b32 v8, 2, v1\n\t"
    "v_add_u32 v8, 160000, v8\n\t"
    "ds_read_b32 v8, v8\n\t"
    "s_waitcnt vmcnt(8)\n\t"
    "s_add_u32 m0, s44, 143360\n\t"
    "s_nop 0\n\t"
    "buffer_load_dwordx4 v23, s[8:11], s47 offen lds\n\t"
    "s_waitcnt vmcnt(8)\n\t"
    "s_add_u32 m0, s44, 147456\n\t"
    "s_nop 0\n\t"
    "buffer_load_dwordx4 v23, s[8:11], s48 offen lds\n\t"
    "s_waitcnt vmcnt(8)\n\t"
    "s_add_u32 m0, s44, 151552\n\t"
    "s_nop 0\n\t"
    "buffer_load_dwordx4 v23, s[8:11], s49 offen lds\n\t"
    "s_waitcnt vmcnt(8)\n\t"
    "s_add_u32 m0, s44, 155648\n\t"
    "s_nop 0\n\t"
    "buffer_load_dwordx4 v23, s[8:11], s50 offen lds\n\t"
    "s_waitcnt vmcnt(8)\n\t"
    "s_add_u32 m0, s44, 0\n\t"
    "s_nop 0\n\t"
    "buffer_load_dwordx4 v23, s[8:11], s51 offen lds\n\t"
    "s_waitcnt vmcnt(8)\n\t"
    "s_add_u32 m0, s44, 4096\n\t"
    "s_nop 0\n\t"
    "buffer_load_dwordx4 v23, s[8:11], s52 offen lds\n\t"
    "s_waitcnt vmcnt(8)\n\t"
    "s_add_u32 m0, s45, 0\n\t"
    "s_mul_i32 s34, s4, 114688\n\t"
    "buffer_load_dwordx4 v10, s[16:19], s34 offen lds\n\t"
    "s_waitcnt vmcnt(8)\n\t"
    "buffer_load_dwordx4 v11, s[16:19], s34 offen offset:1024 lds\n\t"
    "s_waitcnt vmcnt(8)\n\t"
    "buffer_load_dwordx4 v12, s[16:19], s34 offen offset:2048 lds\n\t"
    "s_waitcnt vmcnt(8)\n\t"
    "buffer_load_dwordx4 v13, s[16:19], s34 offen offset:3072 lds\n\t"
    "s_waitcnt vmcnt(8)\n\t"
    "s_add_u32 m0, s45, 4096\n\t"
    "s_mul_i32 s34, s5, 114688\n\t"
    "buffer_load_dwordx4 v10, s[16:19], s34 offen lds\n\t"
    "s_waitcnt vmcnt(8)\n\t"
    "buffer_load_dwordx4 v11, s[16:19], s34 offen offset:1024 lds\n\t"
    "s_waitcnt vmcnt(8)\n\t"
    "buffer_load_dwordx4 v12, s[16:19], s34 offen offset:2048 lds\n\t"
    "s_waitcnt vmcnt(8)\n\t"
    "buffer_load_dwordx4 v13, s[16:19], s34 offen offset:3072 lds\n\t"
    "s_waitcnt lgkmcnt(0)\n\t"
    "s_mov_b32 s40, 0\n\t"
    "L_cf_%=:\n\t"
    "v_subrev_u32 v8, s39, v8\n\t"
    "v_cmp_gt_u32 vcc, 2, v8\n\t"
    "s_add_u32 s40, s40, 1\n\t"
    "s_cmp_gt_u32 s40, 0x20000\n\t"
    "s_cbranch_scc1 L_cfok_%=\n\t"
    "s_and_b32 s66, vcc_lo, 31\n\t"
    "s_cmp_eq_u32 s66, 31\n\t"
    "s_cbranch_scc1 L_cfok_%=\n\t"
    "buffer_load_dword v8, v9, s[20:23], s32 offen sc1\n\t"
    "s_waitcnt vmcnt(0)\n\t"
    "s_branch L_cf_%=\n\t"
    "L_cfok_%=:\n\t"
    "v_cmp_eq_u32 vcc, 1, v8\n\t"
    "s_and_b32 s66, vcc_lo, 31\n\t"
    "s_bcnt1_i32_b32 s66, s66\n\t"
    "s_cmp_ge_u32 s66, 3\n\t"
    "s_cselect_b32 s66, 1, 0\n\t"
    "s_lshl_b32 s36, s66, 13\n\t"
    "s_add_u32 s3, s3, s36\n\t"
    "s_and_b32 s36, s29, 3\n\t"
    "s_lshl_b32 s32, s36, 11\n\t"
    "s_add_u32 s32, s32, s3\n\t"
    "s_add_u32 s32, s32, 1835008\n\t"
    "s_mul_i32 s36, s36, 458752\n\t"
    "s_lshl_b32 s42, s38, 11\n\t"
    "s_add_u32 s42, s42, s3\n\t"
    "s_add_u32 s42, s42, s36\n\t"
    "s_cmp_eq_u32 s38, 0\n\t"
    "s_cselect_b32 s32, s32, 1073741824\n\t"
    "s_cselect_b32 s36, 512, 0\n\t"
    "s_add_u32 s36, s36, 159744\n\t"
    "v_lshl_add_u32 v5, v1, 2, s36\n\t"
    "s_mov_b32 s40, 0\n\t"
    "s_add_u32 s46, s39, 0\n\t"
    "s_add_u32 s47, s39, 1\n\t"
    "s_add_u32 s48, s39, 2\n\t"
    "s_add_u32 s49, s39, 3\n\t"
    "s_lshl_b32 s56, s4, 14\n\t"
    "s_add_u32 s56, s56, s42\n\t"
    "s_lshl_b32 s57, s5, 14\n\t"
    "s_add_u32 s57, s57, s42\n\t"
    "s_lshl_b32 s58, s6, 14\n\t"
    "s_add_u32 s58, s58, s42\n\t"
    "s_lshl_b32 s59, s7, 14\n\t"
    "s_add_u32 s59, s59, s42\n\t"
    "s_lshl_b32 s60, s53, 14\n\t"
    "s_add_u32 s60, s60, s42\n\t"
    "s_lshl_b32 s61, s54, 14\n\t"
    "s_add_u32 s61, s61, s42\n\t"
    "s_lshl_b32 s62, s55, 14\n\t"
    "s_add_u32 s62, s62, s42\n\t"
    "s_waitcnt vmcnt(8)\n\t"
    "s_waitcnt lgkmcnt(0)\n\t"
    "s_barrier\n\t"
    "s_waitcnt vmcnt(8)\n\t"
    "s_add_u32 m0, s45, 8192\n\t"
    "s_mul_i32 s34, s6, 114688\n\t"
    "buffer_load_dwordx4 v10, s[16:19], s34 offen lds\n\t"
    "s_waitcnt vmcnt(8)\n\t"
    "buffer_load_dwordx4 v11, s[16:19], s34 offen offset:1024 lds\n\t"
    "s_waitcnt vmcnt(8)\n\t"
    "buffer_load_dwordx4 v12, s[16:19], s34 offen offset:2048 lds\n\t"
    "s_waitcnt vmcnt(8)\n\t"
    "buffer_load_dwordx4 v13, s[16:19], s34 offen offset:3072 lds\n\t"
    "s_waitcnt vmcnt(8)\n\t"
    "ds_read_b64_tr_b16 a[0:1], v48 offset:0\n\t"
    "ds_read_b64_tr_b16 a[2:3], v48 offset:2048\n\t"
    "ds_read_b64_tr_b16 a[4:5], v49 offset:0\n\t"
    "ds_read_b64_tr_b16 a[6:7], v49 offset:2048\n\t"
    "ds_read_b64_tr_b16 a[8:9], v50 offset:0\n\t"
    "ds_read_b64_tr_b16 a[10:11], v50 offset:2048\n\t"
    "ds_read_b64_tr_b16 a[12:13], v51 offset:0\n\t"
    "ds_read_b64_tr_b16 a[14:15], v51 offset:2048\n\t"
    "s_waitcnt vmcnt(8)\n\t"
    "s_add_u32 m0, s45, 12288\n\t"
    "s_mul_i32 s34, s7, 114688\n\t"
    "buffer_load_dwordx4 v10, s[16:19], s34 offen lds\n\t"
    "s_waitcnt vmcnt(8)\n\t"
    "buffer_load_dwordx4 v11, s[16:19], s34 offen offset:1024 lds\n\t"
    "s_waitcnt vmcnt(8)\n\t"
    "buffer_load_dwordx4 v12, s[16:19], s34 offen offset:2048 lds\n\t"
    "s_waitcnt vmcnt(8)\n\t"
    "buffer_load_dwordx4 v13, s[16:19], s34 offen offset:3072 lds\n\t"
    "s_waitcnt vmcnt(8)\n\t"
    "ds_read_b64_tr_b16 a[16:17], v48 offset:4096\n\t"
    "ds_read_b64_tr_b16 a[18:19], v48 offset:6144\n\t"
    "ds_read_b64_tr_b16 a[20:21], v49 offset:4096\n\t"
    "ds_read_b64_tr_b16 a[22:23], v49 offset:6144\n\t"
    "ds_read_b64_tr_b16 a[24:25], v50 offset:4096\n\t"
    "ds_read_b64_tr_b16 a[26:27], v50 offset:6144\n\t"
    "ds_read_b64_tr_b16 a[28:29], v51 offset:4096\n\t"
    "ds_read_b64_tr_b16 a[30:31], v51 offset:6144\n\t"
    "v_mov_b32 v15, 0\n\t"
    "buffer_atomic_add v15, v3, s[20:23], s32 offen sc0 sc1\n\t"
    "s_waitcnt vmcnt(8)\n\t"
    "s_waitcnt lgkmcnt(8)\n\t"
    "s_add_u32 m0, s45, 0\n\t"
    "s_mul_i32 s34, s53, 114688\n\t"
    "buffer_load_dwordx4 v10, s[16:19], s34 offen lds\n\t"
    "s_waitcnt vmcnt(8)\n\t"
    "buffer_load_dwordx4 v11, s[16:19], s34 offen offset:1024 lds\n\t"
    "s_waitcnt vmcnt(8)\n\t"
    "buffer_load_dwordx4 v12, s[16:19], s34 offen offset:2048 lds\n\t"
    "s_waitcnt vmcnt(8)\n\t"
    "buffer_load_dwordx4 v13, s[16:19], s34 offen offset:3072 lds\n\t"
    "s_waitcnt vmcnt(9)\n\t"
    "ds_read_b64_tr_b16 a[32:33], v48 offset:8192\n\t"
    "ds_read_b64_tr_b16 a[34:35], v48 offset:10240\n\t"
    "ds_read_b64_tr_b16 a[36:37], v49 offset:8192\n\t"
    "ds_read_b64_tr_b16 a[38:39], v49 offset:10240\n\t"
    "ds_read_b64_tr_b16 a[40:41], v50 offset:8192\n\t"
    "ds_read_b64_tr_b16 a[42:43], v50 offset:10240\n\t"
    "ds_read_b64_tr_b16 a[44:45], v51 offset:8192\n\t"
    "ds_read_b64_tr_b16 a[46:47], v51 offset:10240\n\t"
    "s_waitcnt vmcnt(8)\n\t"
    "s_waitcnt lgkmcnt(8)\n\t"
    "s_add_u32 m0, s45, 4096\n\t"
    "s_mul_i32 s34, s54, 114688\n\t"
    "buffer_load_dwordx4 v10, s[16:19], s34 offen lds\n\t"
    "s_waitcnt vmcnt(8)\n\t"
    "buffer_load_dwordx4 v11, s[16:19], s34 offen offset:1024 lds\n\t"
    "s_waitcnt vmcnt(8)\n\t"
    "buffer_load_dwordx4 v12, s[16:19], s34 offen offset:2048 lds\n\t"
    "s_waitcnt vmcnt(8)\n\t"
    "buffer_load_dwordx4 v13, s[16:19], s34 offen offset:3072 lds\n\t"
    "s_waitcnt vmcnt(9)\n\t"
    "ds_read_b64_tr_b16 a[48:49], v48 offset:12288\n\t"
    "ds_read_b64_tr_b16 a[50:51], v48 offset:14336\n\t"
    "ds_read_b64_tr_b16 a[52:53], v49 offset:12288\n\t"
    "ds_read_b64_tr_b16 a[54:55], v49 offset:14336\n\t"
    "ds_read_b64_tr_b16 a[56:57], v50 offset:12288\n\t"
    "ds_read_b64_tr_b16 a[58:59], v50 offset:14336\n\t"
    "ds_read_b64_tr_b16 a[60:61], v51 offset:12288\n\t"
    "ds_read_b64_tr_b16 a[62:63], v51 offset:14336\n\t"
    "s_waitcnt vmcnt(8)\n\t"
    "s_waitcnt lgkmcnt(8)\n\t"
    "s_add_u32 m0, s45, 8192\n\t"
    "s_mul_i32 s34, s55, 114688\n\t"
    "buffer_load_dwordx4 v10, s[16:19], s34 offen lds\n\t"
    "s_waitcnt vmcnt(9)\n\t"
    "s_cmp_lg_u32 s38, 0\n\t"
    "s_cbranch_scc1 L_c0ok_%=\n\t"
    "L_c0_%=:\n\t"
    "v_mov_b32 v4, v15\n\t"
    "v_subrev_u32 v4, s46, v4\n\t"
    "v_cmp_gt_u32 vcc, 4, v4\n\t"
    "s_or_b32 s36, vcc_lo, 0xf0000000\n\t"
    "s_cmp_eq_u32 s36, -1\n\t"
    "s_cbranch_scc0 L_c0rt_%=\n\t"
    "L_c0ok_%=:\n\t"
    "ds_write_b32 v5, v15\n\t"
    "s_waitcnt vmcnt(8)\n\t"
    "buffer_load_dwordx4 v11, s[16:19], s34 offen offset:1024 lds\n\t"
    "s_waitcnt vmcnt(8)\n\t"
    "buffer_load_dwordx4 v12, s[16:19], s34 offen offset:2048 lds\n\t"
    "s_waitcnt vmcnt(8)\n\t"
    "buffer_load_dwordx4 v13, s[16:19], s34 offen offset:3072 lds\n\t"
    "s_waitcnt vmcnt(8)\n\t"
    "ds_read_b64_tr_b16 a[64:65], v48 offset:0\n\t"
    "ds_read_b64_tr_b16 a[66:67], v48 offset:2048\n\t"
    "ds_read_b64_tr_b16 a[68:69], v49 offset:0\n\t"
    "ds_read_b64_tr_b16 a[70:71], v49 offset:2048\n\t"
    "ds_read_b64_tr_b16 a[72:73], v50 offset:0\n\t"
    "ds_read_b64_tr_b16 a[74:75], v50 offset:2048\n\t"
    "ds_read_b64_tr_b16 a[76:77], v51 offset:0\n\t"
    "ds_read_b64_tr_b16 a[78:79], v51 offset:2048\n\t"
    "s_mul_i32 s36, s38, 8704\n\t"
    "s_add_u32 s36, s36, 106496\n\t"
    "v_lshl_add_u32 v10, v1, 2, s36\n\t"
    "s_mov_b32 m0, s45\n\t"
    "s_lshl_b32 s36, s38, 8\n\t"
    "s_add_u32 s36, s36, 161792\n\t"
    "v_lshl_add_u32 v11, v1, 2, s36\n\t"
    "s_waitcnt vmcnt(8)\n\t"
    "ds_read_b32 v4, v14\n\t"
    "s_waitcnt lgkmcnt(0)\n\t"
    "L_chk0_%=:\n\t"
    "v_subrev_u32 v4, s46, v4\n\t"
    "v_cmp_gt_u32 vcc, 4, v4\n\t"
    "s_or_b32 vcc_lo, vcc_lo, 0xf0000000\n\t"
    "s_or_b32 vcc_hi, vcc_hi, 0xf0000000\n\t"
    "s_max_u32 s36, vcc_lo, vcc_hi\n\t"
    "s_cmp_eq_u32 s36, -1\n\t"
    "s_cbranch_scc0 L_rt0_%=\n\t"
    "L_ok0_%=:\n\t"
    "buffer_load_dwordx4 v[80:83], v2, s[20:23], s56 offen offset:0 sc1\n\t"
    "s_waitcnt vmcnt(8)\n\t"
    "buffer_load_dwordx4 v[84:87], v2, s[20:23], s56 offen offset:1024 sc1\n\t"
    "s_waitcnt vmcnt(8)\n\t"
    "buffer_load_dwordx4 v[88:91], v2, s[20:23], s57 offen offset:0 sc1\n\t"
    "s_waitcnt vmcnt(8)\n\t"
    "buffer_load_dwordx4 v[92:95], v2, s[20:23], s57 offen offset:1024 sc1\n\t"
    "s_waitcnt vmcnt(8)\n\t"
    "v_mov_b32 v15, 0\n\t"
    "buffer_atomic_add v15, v3, s[20:23], s32 offen sc0 sc1\n\t"
    "buffer_load_dwordx4 v[96:99], v2, s[20:23], s58 offen offset:0 sc1\n\t"
    "s_waitcnt vmcnt(8)\n\t"
    "buffer_load_dwordx4 v[100:103], v2, s[20:23], s58 offen offset:1024 sc1\n\t"
    "s_waitcnt vmcnt(8)\n\t"
    "buffer_load_dwordx4 v[104:107], v2, s[20:23], s59 offen offset:0 sc1\n\t"
    "s_waitcnt vmcnt(8)\n\t"
    "buffer_load_dwordx4 v[108:111], v2, s[20:23], s59 offen offset:1024 sc1\n\t"
    "s_waitcnt lgkmcnt(15)\n\t"
    "s_waitcnt vmcnt(8)\n\t"
    "v_mfma_f32_16x16x32_bf16 v[16:19], v[80:83], a[0:3], 0\n\t"
    "buffer_load_dwordx4 v[112:115], v2, s[20:23], s60 offen offset:0 sc1\n\t"
    "v_mfma_f32_16x16x32_bf16 v[20:23], v[80:83], a[4:7], 0\n\t"
    "v_mfma_f32_16x16x32_bf16 v[24:27], v[80:83], a[8:11], 0\n\t"
    "v_mfma_f32_16x16x32_bf16 v[28:31], v[80:83], a[12:15], 0\n\t"
    "s_waitcnt vmcnt(8)\n\t"
    "v_mfma_f32_16x16x32_bf16 v[32:35], v[84:87], a[0:3], 0\n\t"
    "buffer_load_dwordx4 v[116:119], v2, s[20:23], s60 offen offset:1024 sc1\n\t"
    "v_mfma_f32_16x16x32_bf16 v[36:39], v[84:87], a[4:7], 0\n\t"
    "v_mfma_f32_16x16x32_bf16 v[40:43], v[84:87], a[8:11], 0\n\t"
    "v_mfma_f32_16x16x32_bf16 v[44:47], v[84:87], a[12:15], 0\n\t"
    "s_waitcnt lgkmcnt(15)\n\t"
    "s_waitcnt vmcnt(8)\n\t"
    "v_mfma_f32_16x16x32_bf16 v[16:19], v[88:91], a[16:19], v[16:19]\n\t"
    "buffer_load_dwordx4 v[120:123], v2, s[20:23], s61 offen offset:0 sc1\n\t"
    "v_mfma_f32_16x16x32_bf16 v[20:23], v[88:91], a[20:23], v[20:23]\n\t"
    "v_mfma_f32_16x16x32_bf16 v[24:27], v[88:91], a[24:27], v[24:27]\n\t"
    "v_mfma_f32_16x16x32_bf16 v[28:31], v[88:91], a[28:31], v[28:31]\n\t"
    "s_waitcnt vmcnt(8)\n\t"
    "v_mfma_f32_16x16x32_bf16 v[32:35], v[92:95], a[16:19], v[32:35]\n\t"
    "buffer_load_dwordx4 v[124:127], v2, s[20:23], s61 offen offset:1024 sc1\n\t"
    "v_mfma_f32_16x16x32_bf16 v[36:39], v[92:95], a[20:23], v[36:39]\n\t"
    "v_mfma_f32_16x16x32_bf16 v[40:43], v[92:95], a[24:27], v[40:43]\n\t"
    "v_mfma_f32_16x16x32_bf16 v[44:47], v[92:95], a[28:31], v[44:47]\n\t"
    "s_waitcnt vmcnt(17)\n\t"
    "s_waitcnt lgkmcnt(15)\n\t"
    "s_waitcnt vmcnt(7)\n\t"
    "v_mfma_f32_16x16x32_bf16 v[16:19], v[96:99], a[32:35], v[16:19]\n\t"
    "buffer_load_dwordx4 v[128:131], v2, s[20:23], s62 offen offset:0 sc1\n\t"
    "v_mfma_f32_16x16x32_bf16 v[20:23], v[96:99], a[36:39], v[20:23]\n\t"
    "ds_read_b64_tr_b16 a[80:81], v48 offset:4096\n\t"
    "v_mfma_f32_16x16x32_bf16 v[24:27], v[96:99], a[40:43], v[24:27]\n\t"
    "ds_read_b64_tr_b16 a[82:83], v48 offset:6144\n\t"
    "v_mfma_f32_16x16x32_bf16 v[28:31], v[96:99], a[44:47], v[28:31]\n\t"
    "ds_read_b64_tr_b16 a[84:85], v49 offset:4096\n\t"
    "s_waitcnt vmcnt(7)\n\t"
    "v_mfma_f32_16x16x32_bf16 v[32:35], v[100:103], a[32:35], v[32:35]\n\t"
    "s_waitcnt vmcnt(9)\n\t"
    "ds_write_b32 v5, v15\n\t"
    "buffer_load_dwordx4 v[132:135], v2, s[20:23], s62 offen offset:1024 sc1\n\t"
    "v_add_u32 v2, 114688, v2\n\t"
    "ds_read_b32 v4, v14\n\t"
    "v_mfma_f32_16x16x32_bf16 v[36:39], v[100:103], a[36:39], v[36:39]\n\t"
    "ds_read_b64_tr_b16 a[86:87], v49 offset:6144\n\t"
    "v_mfma_f32_16x16x32_bf16 v[40:43], v[100:103], a[40:43], v[40:43]\n\t"
    "ds_read_b64_tr_b16 a[88:89], v50 offset:4096\n\t"
    "v_mfma_f32_16x16x32_bf16 v[44:47], v[100:103], a[44:47], v[44:47]\n\t"
    "ds_read_b64_tr_b16 a[90:91], v50 offset:6144\n\t"
    "s_waitcnt vmcnt(15)\n\t"
    "s_waitcnt lgkmcnt(15)\n\t"
    "s_waitcnt vmcnt(7)\n\t"
    "v_mfma_f32_16x16x32_bf16 v[16:19], v[104:107], a[48:51], v[16:19]\n\t"
    "s_waitcnt lgkmcnt(3)\n\t"
    "L_chk1_%=:\n\t"
    "v_subrev_u32 v4, s47, v4\n\t"
    "v_cmp_gt_u32 vcc, 3, v4\n\t"
    "s_or_b32 vcc_lo, vcc_lo, 0xf0000000\n\t"
    "s_or_b32 vcc_hi, vcc_hi, 0xf0000000\n\t"
    "s_max_u32 s36, vcc_lo, vcc_hi\n\t"
    "s_cmp_eq_u32 s36, -1\n\t"
    "s_cbranch_scc0 L_rt1_%=\n\t"
    "L_ok1_%=:\n\t"
    "buffer_load_dwordx4 v[136:139], v2, s[20:23], s57 offen offset:0 sc1\n\t"
    "v_mfma_f32_16x16x32_bf16 v[20:23], v[104:107], a[52:55], v[20:23]\n\t"
    "ds_read_b64_tr_b16 a[92:93], v51 offset:4096\n\t"
    "v_mfma_f32_16x16x32_bf16 v[24:27], v[104:107], a[56:59], v[24:27]\n\t"
    "ds_read_b64_tr_b16 a[94:95], v51 offset:6144\n\t"
    "v_mfma_f32_16x16x32_bf16 v[28:31], v[104:107], a[60:63], v[28:31]\n\t"
    "ds_read_b64_tr_b16 a[96:97], v48 offset:8192\n\t"
    "s_waitcnt vmcnt(7)\n\t"
    "v_mfma_f32_16x16x32_bf16 v[32:35], v[108:111], a[48:51], v[32:35]\n\t"
    "buffer_load_dwordx4 v[140:143], v2, s[20:23], s57 offen offset:1024 sc1\n\t"
    "v_mfma_f32_16x16x32_bf16 v[36:39], v[108:111], a[52:55], v[36:39]\n\t"
    "ds_read_b64_tr_b16 a[98:99], v48 offset:10240\n\t"
    "v_mfma_f32_16x16x32_bf16 v[40:43], v[108:111], a[56:59], v[40:43]\n\t"
    "ds_read_b64_tr_b16 a[100:101], v49 offset:8192\n\t"
    "v_mfma_f32_16x16x32_bf16 v[44:47], v[108:111], a[60:63], v[44:47]\n\t"
    "ds_read_b64_tr_b16 a[102:103], v49 offset:10240\n\t"
    "s_waitcnt lgkmcnt(15)\n\t"
    "s_waitcnt vmcnt(7)\n\t"
    "v_mfma_f32_16x16x32_bf16 v[16:19], v[112:115], a[64:67], v[16:19]\n\t"
    "buffer_load_dwordx4 v[80:83], v2, s[20:23], s58 offen offset:0 sc1\n\t"
    "v_mfma_f32_16x16x32_bf16 v[20:23], v[112:115], a[68:71], v[20:23]\n\t"
    "ds_read_b64_tr_b16 a[104:105], v50 offset:8192\n\t"
    "v_mfma_f32_16x16x32_bf16 v[24:27], v[112:115], a[72:75], v[24:27]\n\t"
    "ds_read_b64_tr_b16 a[106:107], v50 offset:10240\n\t"
    "v_mfma_f32_16x16x32_bf16 v[28:31], v[112:115], a[76:79], v[28:31]\n\t"
    "ds_read_b64_tr_b16 a[108:109], v51 offset:8192\n\t"
    "s_waitcnt vmcnt(7)\n\t"
    "v_mfma_f32_16x16x32_bf16 v[32:35], v[116:119], a[64:67], v[32:35]\n\t"
    "buffer_load_dwordx4 v[84:87], v2, s[20:23], s58 offen offset:1024 sc1\n\t"
    "v_mfma_f32_16x16x32_bf16 v[36:39], v[116:119], a[68:71], v[36:39]\n\t"
    "ds_read_b64_tr_b16 a[110:111], v51 offset:10240\n\t"
    "v_mfma_f32_16x16x32_bf16 v[40:43], v[116:119], a[72:75], v[40:43]\n\t"
    "v_mfma_f32_16x16x32_bf16 v[44:47], v[116:119], a[76:79], v[44:47]\n\t"
    "s_waitcnt lgkmcnt(8)\n\t"
    "s_waitcnt vmcnt(7)\n\t"
    "v_mfma_f32_16x16x32_bf16 v[16:19], v[120:123], a[80:83], v[16:19]\n\t"
    "buffer_load_dwordx4 v[88:91], v2, s[20:23], s59 offen offset:0 sc1\n\t"
    "v_mfma_f32_16x16x32_bf16 v[20:23], v[120:123], a[84:87], v[20:23]\n\t"
    "v_mfma_f32_16x16x32_bf16 v[24:27], v[120:123], a[88:91], v[24:27]\n\t"
    "v_mfma_f32_16x16x32_bf16 v[28:31], v[120:123], a[92:95], v[28:31]\n\t"
    "s_waitcnt vmcnt(7)\n\t"
    "v_mfma_f32_16x16x32_bf16 v[32:35], v[124:127], a[80:83], v[32:35]\n\t"
    "buffer_load_dwordx4 v[92:95], v2, s[20:23], s59 offen offset:1024 sc1\n\t"
    "v_mfma_f32_16x16x32_bf16 v[36:39], v[124:127], a[84:87], v[36:39]\n\t"
    "v_mfma_f32_16x16x32_bf16 v[40:43], v[124:127], a[88:91], v[40:43]\n\t"
    "v_mfma_f32_16x16x32_bf16 v[44:47], v[124:127], a[92:95], v[44:47]\n\t"
    "s_waitcnt lgkmcnt(0)\n\t"
    "s_waitcnt vmcnt(7)\n\t"
    "v_mfma_f32_16x16x32_bf16 v[16:19], v[128:131], a[96:99], v[16:19]\n\t"
    "buffer_load_dwordx4 v[96:99], v2, s[20:23], s60 offen offset:0 sc1\n\t"
    "v_mfma_f32_16x16x32_bf16 v[20:23], v[128:131], a[100:103], v[20:23]\n\t"
    "v_mfma_f32_16x16x32_bf16 v[24:27], v[128:131], a[104:107], v[24:27]\n\t"
    "v_mfma_f32_16x16x32_bf16 v[28:31], v[128:131], a[108:111], v[28:31]\n\t"
    "s_waitcnt vmcnt(7)\n\t"
    "v_mfma_f32_16x16x32_bf16 v[32:35], v[132:135], a[96:99], v[32:35]\n\t"
    "buffer_load_dwordx4 v[100:103], v2, s[20:23], s60 offen offset:1024 sc1\n\t"
    "v_mfma_f32_16x16x32_bf16 v[36:39], v[132:135], a[100:103], v[36:39]\n\t"
    "v_mfma_f32_16x16x32_bf16 v[40:43], v[132:135], a[104:107], v[40:43]\n\t"
    "v_mfma_f32_16x16x32_bf16 v[44:47], v[132:135], a[108:111], v[44:47]\n\t"
    "s_waitcnt vmcnt(7)\n\t"
    "v_mfma_f32_16x16x32_bf16 v[48:51], v[136:139], a[16:19], 0\n\t"
    "buffer_load_dwordx4 v[104:107], v2, s[20:23], s61 offen offset:0 sc1\n\t"
    "ds_write_addtid_b32 v16 offset:0\n\t"
    "ds_write_addtid_b32 v17 offset:272\n\t"
    "v_mfma_f32_16x16x32_bf16 v[52:55], v[136:139], a[20:23], 0\n\t"
    "ds_write_addtid_b32 v18 offset:544\n\t"
    "ds_write_addtid_b32 v19 offset:816\n\t"
    "v_mfma_f32_16x16x32_bf16 v[56:59], v[136:139], a[24:27], 0\n\t"
    "ds_write_addtid_b32 v20 offset:1088\n\t"
    "ds_write_addtid_b32 v21 offset:1360\n\t"
    "v_mfma_f32_16x16x32_bf16 v[60:63], v[136:139], a[28:31], 0\n\t"
    "ds_write_addtid_b32 v22 offset:1632\n\t"
    "ds_write_addtid_b32 v23 offset:1904\n\t"
    "s_waitcnt vmcnt(7)\n\t"
    "v_mfma_f32_16x16x32_bf16 v[64:67], v[140:143], a[16:19], 0\n\t"
    "buffer_load_dwordx4 v[108:111], v2, s[20:23], s62 offen offset:0 sc1\n\t"
    "ds_write_addtid_b32 v24 offset:2176\n\t"
    "ds_write_addtid_b32 v25 offset:2448\n\t"
    "v_mfma_f32_16x16x32_bf16 v[68:71], v[140:143], a[20:23], 0\n\t"
    "ds_write_addtid_b32 v26 offset:2720\n\t"
    "ds_write_addtid_b32 v27 offset:2992\n\t"
    "v_mfma_f32_16x16x32_bf16 v[72:75], v[140:143], a[24:27], 0\n\t"
    "ds_write_addtid_b32 v28 offset:3264\n\t"
    "ds_write_addtid_b32 v29 offset:3536\n\t"
    "v_mfma_f32_16x16x32_bf16 v[76:79], v[140:143], a[28:31], 0\n\t"
    "ds_write_addtid_b32 v30 offset:3808\n\t"
    "ds_write_addtid_b32 v31 offset:4080\n\t"
    "s_waitcnt vmcnt(7)\n\t"
    "v_mfma_f32_16x16x32_bf16 v[48:51], v[80:83], a[32:35], v[48:51]\n\t"
    "ds_write_addtid_b32 v32 offset:4352\n\t"
    "ds_write_addtid_b32 v33 offset:4624\n\t"
    "v_mfma_f32_16x16x32_bf16 v[52:55], v[80:83], a[36:39], v[52:55]\n\t"
    "ds_write_addtid_b32 v34 offset:4896\n\t"
    "ds_write_addtid_b32 v35 offset:5168\n\t"
    "v_mfma_f32_16x16x32_bf16 v[56:59], v[80:83], a[40:43], v[56:59]\n\t"
    "ds_write_addtid_b32 v36 offset:5440\n\t"
    "ds_write_addtid_b32 v37 offset:5712\n\t"
    "v_mfma_f32_16x16x32_bf16 v[60:63], v[80:83], a[44:47], v[60:63]\n\t"
    "ds_write_addtid_b32 v38 offset:5984\n\t"
    "ds_write_addtid_b32 v39 offset:6256\n\t"
    "s_waitcnt vmcnt(6)\n\t"
    "v_mfma_f32_16x16x32_bf16 v[64:67], v[84:87], a[32:35], v[64:67]\n\t"
    "buffer_load_dwordx4 v[116:119], v2, s[20:23], s61 offen offset:1024 sc1\n\t"
    "ds_write_addtid_b32 v40 offset:6528\n\t"
    "ds_write_addtid_b32 v41 offset:6800\n\t"
    "v_mfma_f32_16x16x32_bf16 v[68:71], v[84:87], a[36:39], v[68:71]\n\t"
    "ds_write_addtid_b32 v42 offset:7072\n\t"
    "ds_write_addtid_b32 v43 offset:7344\n\t"
    "v_mfma_f32_16x16x32_bf16 v[72:75], v[84:87], a[40:43], v[72:75]\n\t"
    "ds_write_addtid_b32 v44 offset:7616\n\t"
    "ds_write_addtid_b32 v45 offset:7888\n\t"
    "v_mfma_f32_16x16x32_bf16 v[76:79], v[84:87], a[44:47], v[76:79]\n\t"
    "ds_write_addtid_b32 v46 offset:8160\n\t"
    "ds_write_addtid_b32 v47 offset:8432\n\t"
    "v_mov_b32 v12, 1\n\t"
    "ds_write_b32 v11, v12\n\t"
    "s_waitcnt vmcnt(6)\n\t"
    "v_mfma_f32_16x16x32_bf16 v[48:51], v[88:91], a[48:51], v[48:51]\n\t"
    "buffer_load_dwordx4 v[120:123], v2, s[20:23], s62 offen offset:1024 sc1\n\t"
    "v_add_u32 v2, 114688, v2\n\t"
    "ds_read_b32 v4, v14\n\t"
    "v_mfma_f32_16x16x32_bf16 v[52:55], v[88:91], a[52:55], v[52:55]\n\t"
    "v_mfma_f32_16x16x32_bf16 v[56:59], v[88:91], a[56:59], v[56:59]\n\t"
    "v_mfma_f32_16x16x32_bf16 v[60:63], v[88:91], a[60:63], v[60:63]\n\t"
    "s_waitcnt vmcnt(6)\n\t"
    "v_mfma_f32_16x16x32_bf16 v[64:67], v[92:95], a[48:51], v[64:67]\n\t"
    "v_mfma_f32_16x16x32_bf16 v[68:71], v[92:95], a[52:55], v[68:71]\n\t"
    "v_mfma_f32_16x16x32_bf16 v[72:75], v[92:95], a[56:59], v[72:75]\n\t"
    "v_mfma_f32_16x16x32_bf16 v[76:79], v[92:95], a[60:63], v[76:79]\n\t"
    "s_waitcnt vmcnt(5)\n\t"
    "v_mfma_f32_16x16x32_bf16 v[48:51], v[96:99], a[64:67], v[48:51]\n\t"
    "v_mfma_f32_16x16x32_bf16 v[52:55], v[96:99], a[68:71], v[52:55]\n\t"
    "v_mfma_f32_16x16x32_bf16 v[56:59], v[96:99], a[72:75], v[56:59]\n\t"
    "v_mfma_f32_16x16x32_bf16 v[60:63], v[96:99], a[76:79], v[60:63]\n\t"
    "s_waitcnt vmcnt(4)\n\t"
    "v_mfma_f32_16x16x32_bf16 v[64:67], v[100:103], a[64:67], v[64:67]\n\t"
    "v_mfma_f32_16x16x32_bf16 v[68:71], v[100:103], a[68:71], v[68:71]\n\t"
    "v_mfma_f32_16x16x32_bf16 v[72:75], v[100:103], a[72:75], v[72:75]\n\t"
    "v_mfma_f32_16x16x32_bf16 v[76:79], v[100:103], a[76:79], v[76:79]\n\t"
    "s_waitcnt vmcnt(3)\n\t"
    "v_mfma_f32_16x16x32_bf16 v[48:51], v[104:107], a[80:83], v[48:51]\n\t"
    "s_waitcnt lgkmcnt(0)\n\t"
    "L_chk2_%=:\n\t"
    "v_subrev_u32 v4, s48, v4\n\t"
    "v_cmp_gt_u32 vcc, 2, v4\n\t"
    "s_max_u32 s36, vcc_lo, vcc_hi\n\t"
    "s_cmp_eq_u32 s36, -1\n\t"
    "s_cbranch_scc0 L_rt2_%=\n\t"
    "L_ok2_%=:\n\t"
    "buffer_load_dwordx4 v[136:139], v2, s[20:23], s57 offen offset:0 sc1\n\t"
    "v_mfma_f32_16x16x32_bf16 v[52:55], v[104:107], a[84:87], v[52:55]\n\t"
    "v_mfma_f32_16x16x32_bf16 v[56:59], v[104:107], a[88:91], v[56:59]\n\t"
    "ds_read_b128 v[112:115], v6 offset:8192\n\t"
    "v_mfma_f32_16x16x32_bf16 v[60:63], v[104:107], a[92:95], v[60:63]\n\t"
    "s_waitcnt vmcnt(3)\n\t"
    "v_mfma_f32_16x16x32_bf16 v[48:51], v[108:111], a[96:99], v[48:51]\n\t"
    "buffer_load_dwordx4 v[140:143], v2, s[20:23], s57 offen offset:1024 sc1\n\t"
    "v_mfma_f32_16x16x32_bf16 v[52:55], v[108:111], a[100:103], v[52:55]\n\t"
    "v_mfma_f32_16x16x32_bf16 v[56:59], v[108:111], a[104:107], v[56:59]\n\t"
    "v_mfma_f32_16x16x32_bf16 v[60:63], v[108:111], a[108:111], v[60:63]\n\t"
    "s_waitcnt lgkmcnt(0)\n\t"
    "v_mfma_f32_16x16x32_bf16 v[48:51], v[112:115], a[0:3], v[48:51]\n\t"
    "buffer_load_dwordx4 v[80:83], v2, s[20:23], s58 offen offset:0 sc1\n\t"
    "v_mfma_f32_16x16x32_bf16 v[52:55], v[112:115], a[4:7], v[52:55]\n\t"
    "v_mfma_f32_16x16x32_bf16 v[56:59], v[112:115], a[8:11], v[56:59]\n\t"
    "v_mfma_f32_16x16x32_bf16 v[60:63], v[112:115], a[12:15], v[60:63]\n\t"
    "s_waitcnt vmcnt(4)\n\t"
    "v_mfma_f32_16x16x32_bf16 v[64:67], v[116:119], a[80:83], v[64:67]\n\t"
    "buffer_load_dwordx4 v[84:87], v2, s[20:23], s58 offen offset:1024 sc1\n\t"
    "v_mfma_f32_16x16x32_bf16 v[68:71], v[116:119], a[84:87], v[68:71]\n\t"
    "v_mfma_f32_16x16x32_bf16 v[72:75], v[116:119], a[88:91], v[72:75]\n\t"
    "ds_read_b128 v[124:127], v6 offset:9216\n\t"
    "v_mfma_f32_16x16x32_bf16 v[76:79], v[116:119], a[92:95], v[76:79]\n\t"
    "s_waitcnt vmcnt(4)\n\t"
    "v_mfma_f32_16x16x32_bf16 v[64:67], v[120:123], a[96:99], v[64:67]\n\t"
    "buffer_load_dwordx4 v[88:91], v2, s[20:23], s59 offen offset:0 sc1\n\t"
    "ds_write_addtid_b32 v48 offset:8704\n\t"
    "ds_write_addtid_b32 v49 offset:8976\n\t"
    "v_mfma_f32_16x16x32_bf16 v[68:71], v[120:123], a[100:103], v[68:71]\n\t"
    "ds_write_addtid_b32 v50 offset:9248\n\t"
    "ds_write_addtid_b32 v51 offset:9520\n\t"
    "v_mfma_f32_16x16x32_bf16 v[72:75], v[120:123], a[104:107], v[72:75]\n\t"
    "ds_read_b128 v[128:131], v6 offset:16384\n\t"
    "ds_write_addtid_b32 v52 offset:9792\n\t"
    "ds_write_addtid_b32 v53 offset:10064\n\t"
    "v_mfma_f32_16x16x32_bf16 v[76:79], v[120:123], a[108:111], v[76:79]\n\t"
    "ds_write_addtid_b32 v54 offset:10336\n\t"
    "ds_write_addtid_b32 v55 offset:10608\n\t"
    "s_waitcnt lgkmcnt(9)\n\t"
    "v_mfma_f32_16x16x32_bf16 v[64:67], v[124:127], a[0:3], v[64:67]\n\t"
    "buffer_load_dwordx4 v[92:95], v2, s[20:23], s59 offen offset:1024 sc1\n\t"
    "ds_write_addtid_b32 v56 offset:10880\n\t"
    "ds_write_addtid_b32 v57 offset:11152\n\t"
    "v_mfma_f32_16x16x32_bf16 v[68:71], v[124:127], a[4:7], v[68:71]\n\t"
    "ds_write_addtid_b32 v58 offset:11424\n\t"
    "ds_write_addtid_b32 v59 offset:11696\n\t"
    "v_mfma_f32_16x16x32_bf16 v[72:75], v[124:127], a[8:11], v[72:75]\n\t"
    "ds_read_b128 v[132:135], v6 offset:17408\n\t"
    "ds_write_addtid_b32 v60 offset:11968\n\t"
    "ds_write_addtid_b32 v61 offset:12240\n\t"
    "v_mfma_f32_16x16x32_bf16 v[76:79], v[124:127], a[12:15], v[76:79]\n\t"
    "ds_write_addtid_b32 v62 offset:12512\n\t"
    "ds_write_addtid_b32 v63 offset:12784\n\t"
    "s_waitcnt lgkmcnt(15)\n\t"
    "s_waitcnt lgkmcnt(13)\n\t"
    "v_mfma_f32_16x16x32_bf16 v[16:19], v[128:131], a[0:3], 0\n\t"
    "buffer_load_dwordx4 v[96:99], v2, s[20:23], s60 offen offset:0 sc1\n\t"
    "v_mfma_f32_16x16x32_bf16 v[20:23], v[128:131], a[4:7], 0\n\t"
    "v_mfma_f32_16x16x32_bf16 v[24:27], v[128:131], a[8:11], 0\n\t"
    "v_mfma_f32_16x16x32_bf16 v[28:31], v[128:131], a[12:15], 0\n\t"
    "s_waitcnt lgkmcnt(4)\n\t"
    "v_mfma_f32_16x16x32_bf16 v[32:35], v[132:135], a[0:3], 0\n\t"
    "buffer_load_dwordx4 v[100:103], v2, s[20:23], s60 offen offset:1024 sc1\n\t"
    "ds_write_addtid_b32 v64 offset:13056\n\t"
    "ds_write_addtid_b32 v65 offset:13328\n\t"
    "v_mfma_f32_16x16x32_bf16 v[36:39], v[132:135], a[4:7], 0\n\t"
    "ds_write_addtid_b32 v66 offset:13600\n\t"
    "ds_write_addtid_b32 v67 offset:13872\n\t"
    "v_mfma_f32_16x16x32_bf16 v[40:43], v[132:135], a[8:11], 0\n\t"
    "ds_write_addtid_b32 v68 offset:14144\n\t"
    "ds_write_addtid_b32 v69 offset:14416\n\t"
    "v_mfma_f32_16x16x32_bf16 v[44:47], v[132:135], a[12:15], 0\n\t"
    "ds_write_addtid_b32 v70 offset:14688\n\t"
    "ds_write_addtid_b32 v71 offset:14960\n\t"
    "s_waitcnt vmcnt(7)\n\t"
    "v_mfma_f32_16x16x32_bf16 v[16:19], v[136:139], a[16:19], v[16:19]\n\t"
    "buffer_load_dwordx4 v[104:107], v2, s[20:23], s61 offen offset:0 sc1\n\t"
    "ds_write_addtid_b32 v72 offset:15232\n\t"
    "ds_write_addtid_b32 v73 offset:15504\n\t"
    "v_mfma_f32_16x16x32_bf16 v[20:23], v[136:139], a[20:23], v[20:23]\n\t"
    "ds_write_addtid_b32 v74 offset:15776\n\t"
    "ds_write_addtid_b32 v75 offset:16048\n\t"
    "v_mfma_f32_16x16x32_bf16 v[24:27], v[136:139], a[24:27], v[24:27]\n\t"
    "ds_write_addtid_b32 v76 offset:16320\n\t"
    "ds_write_addtid_b32 v77 offset:16592\n\t"
    "v_mfma_f32_16x16x32_bf16 v[28:31], v[136:139], a[28:31], v[28:31]\n\t"
    "ds_write_addtid_b32 v78 offset:16864\n\t"
    "ds_write_addtid_b32 v79 offset:17136\n\t"
    "v_mov_b32 v12, 2\n\t"
    "ds_write_b32 v11, v12\n\t"
    "s_waitcnt vmcnt(7)\n\t"
    "v_mfma_f32_16x16x32_bf16 v[32:35], v[140:143], a[16:19], v[32:35]\n\t"
    "buffer_load_dwordx4 v[108:111], v2, s[20:23], s61 offen offset:1024 sc1\n\t"
    "v_mfma_f32_16x16x32_bf16 v[36:39], v[140:143], a[20:23], v[36:39]\n\t"
    "v_mfma_f32_16x16x32_bf16 v[40:43], v[140:143], a[24:27], v[40:43]\n\t"
    "v_mfma_f32_16x16x32_bf16 v[44:47], v[140:143], a[28:31], v[44:47]\n\t"
    "s_waitcnt vmcnt(7)\n\t"
    "v_mfma_f32_16x16x32_bf16 v[16:19], v[80:83], a[32:35], v[16:19]\n\t"
    "buffer_load_dwordx4 v[112:115], v2, s[20:23], s62 offen offset:0 sc1\n\t"
    "v_mfma_f32_16x16x32_bf16 v[20:23], v[80:83], a[36:39], v[20:23]\n\t"
    "v_mfma_f32_16x16x32_bf16 v[24:27], v[80:83], a[40:43], v[24:27]\n\t"
    "v_mfma_f32_16x16x32_bf16 v[28:31], v[80:83], a[44:47], v[28:31]\n\t"
    "s_waitcnt vmcnt(7)\n\t"
    "v_mfma_f32_16x16x32_bf16 v[32:35], v[84:87], a[32:35], v[32:35]\n\t"
    "buffer_load_dwordx4 v[116:119], v2, s[20:23], s62 offen offset:1024 sc1\n\t"
    "v_add_u32 v2, 114688, v2\n\t"
    "ds_read_b32 v4, v14\n\t"
    "v_mfma_f32_16x16x32_bf16 v[36:39], v[84:87], a[36:39], v[36:39]\n\t"
    "v_mfma_f32_16x16x32_bf16 v[40:43], v[84:87], a[40:43], v[40:43]\n\t"
    "v_mfma_f32_16x16x32_bf16 v[44:47], v[84:87], a[44:47], v[44:47]\n\t"
    "s_waitcnt vmcnt(7)\n\t"
    "v_mfma_f32_16x16x32_bf16 v[16:19], v[88:91], a[48:51], v[16:19]\n\t"
    "v_mfma_f32_16x16x32_bf16 v[20:23], v[88:91], a[52:55], v[20:23]\n\t"
    "v_mfma_f32_16x16x32_bf16 v[24:27], v[88:91], a[56:59], v[24:27]\n\t"
    "v_mfma_f32_16x16x32_bf16 v[28:31], v[88:91], a[60:63], v[28:31]\n\t"
    "s_waitcnt vmcnt(6)\n\t"
    "v_mfma_f32_16x16x32_bf16 v[32:35], v[92:95], a[48:51], v[32:35]\n\t"
    "v_mfma_f32_16x16x32_bf16 v[36:39], v[92:95], a[52:55], v[36:39]\n\t"
    "v_mfma_f32_16x16x32_bf16 v[40:43], v[92:95], a[56:59], v[40:43]\n\t"
    "v_mfma_f32_16x16x32_bf16 v[44:47], v[92:95], a[60:63], v[44:47]\n\t"
    "s_waitcnt vmcnt(5)\n\t"
    "v_mfma_f32_16x16x32_bf16 v[16:19], v[96:99], a[64:67], v[16:19]\n\t"
    "s_waitcnt lgkmcnt(0)\n\t"
    "L_chk3_%=:\n\t"
    "v_subrev_u32 v4, s49, v4\n\t"
    "v_cmp_gt_u32 vcc, 1, v4\n\t"
    "s_or_b32 vcc_lo, vcc_lo, 0xf0000000\n\t"
    "s_or_b32 vcc_hi, vcc_hi, 0xf0000000\n\t"
    "s_max_u32 s36, vcc_lo, vcc_hi\n\t"
    "s_cmp_eq_u32 s36, -1\n\t"
    "s_cbranch_scc0 L_rt3_%=\n\t"
    "L_ok3_%=:\n\t"
    "buffer_load_dwordx4 v[128:131], v2, s[20:23], s57 offen offset:0 sc1\n\t"
    "v_mfma_f32_16x16x32_bf16 v[20:23], v[96:99], a[68:71], v[20:23]\n\t"
    "v_mfma_f32_16x16x32_bf16 v[24:27], v[96:99], a[72:75], v[24:27]\n\t"
    "v_mfma_f32_16x16x32_bf16 v[28:31], v[96:99], a[76:79], v[28:31]\n\t"
    "s_waitcnt vmcnt(5)\n\t"
    "v_mfma_f32_16x16x32_bf16 v[32:35], v[100:103], a[64:67], v[32:35]\n\t"
    "buffer_load_dwordx4 v[132:135], v2, s[20:23], s57 offen offset:1024 sc1\n\t"
    "v_mfma_f32_16x16x32_bf16 v[36:39], v[100:103], a[68:71], v[36:39]\n\t"
    "v_mfma_f32_16x16x32_bf16 v[40:43], v[100:103], a[72:75], v[40:43]\n\t"
    "v_mfma_f32_16x16x32_bf16 v[44:47], v[100:103], a[76:79], v[44:47]\n\t"
    "s_waitcnt vmcnt(5)\n\t"
    "v_mfma_f32_16x16x32_bf16 v[16:19], v[104:107], a[80:83], v[16:19]\n\t"
    "buffer_load_dwordx4 v[136:139], v2, s[20:23], s58 offen offset:0 sc1\n\t"
    "v_mfma_f32_16x16x32_bf16 v[20:23], v[104:107], a[84:87], v[20:23]\n\t"
    "v_mfma_f32_16x16x32_bf16 v[24:27], v[104:107], a[88:91], v[24:27]\n\t"
    "v_mfma_f32_16x16x32_bf16 v[28:31], v[104:107], a[92:95], v[28:31]\n\t"
    "s_waitcnt vmcnt(5)\n\t"
    "v_mfma_f32_16x16x32_bf16 v[32:35], v[108:111], a[80:83], v[32:35]\n\t"
    "buffer_load_dwordx4 v[140:143], v2, s[20:23], s58 offen offset:1024 sc1\n\t"
    "v_mfma_f32_16x16x32_bf16 v[36:39], v[108:111], a[84:87], v[36:39]\n\t"
    "v_mfma_f32_16x16x32_bf16 v[40:43], v[108:111], a[88:91], v[40:43]\n\t"
    "v_mfma_f32_16x16x32_bf16 v[44:47], v[108:111], a[92:95], v[44:47]\n\t"
    "s_waitcnt vmcnt(5)\n\t"
    "v_mfma_f32_16x16x32_bf16 v[16:19], v[112:115], a[96:99], v[16:19]\n\t"
    "buffer_load_dwordx4 v[80:83], v2, s[20:23], s59 offen offset:0 sc1\n\t"
    "v_mfma_f32_16x16x32_bf16 v[20:23], v[112:115], a[100:103], v[20:23]\n\t"
    "v_mfma_f32_16x16x32_bf16 v[24:27], v[112:115], a[104:107], v[24:27]\n\t"
    "ds_read_b128 v[120:123], v6 offset:24576\n\t"
    "v_mfma_f32_16x16x32_bf16 v[28:31], v[112:115], a[108:111], v[28:31]\n\t"
    "s_waitcnt vmcnt(5)\n\t"
    "v_mfma_f32_16x16x32_bf16 v[32:35], v[116:119], a[96:99], v[32:35]\n\t"
    "buffer_load_dwordx4 v[84:87], v2, s[20:23], s60 offen offset:0 sc1\n\t"
    "v_mfma_f32_16x16x32_bf16 v[36:39], v[116:119], a[100:103], v[36:39]\n\t"
    "ds_read_b32 v4, v14\n\t"
    "v_mfma_f32_16x16x32_bf16 v[40:43], v[116:119], a[104:107], v[40:43]\n\t"
    "ds_read_b128 v[124:127], v6 offset:25600\n\t"
    "v_mfma_f32_16x16x32_bf16 v[44:47], v[116:119], a[108:111], v[44:47]\n\t"
    "s_waitcnt lgkmcnt(5)\n\t"
    "s_waitcnt lgkmcnt(2)\n\t"
    "v_mfma_f32_16x16x32_bf16 v[48:51], v[120:123], a[0:3], 0\n\t"
    "buffer_load_dwordx4 v[88:91], v2, s[20:23], s61 offen offset:0 sc1\n\t"
    "ds_write_b32 v10, v16 offset:0\n\t"
    "ds_write_b32 v10, v17 offset:272\n\t"
    "v_mfma_f32_16x16x32_bf16 v[52:55], v[120:123], a[4:7], 0\n\t"
    "ds_write_b32 v10, v18 offset:544\n\t"
    "ds_write_b32 v10, v19 offset:816\n\t"
    "v_mfma_f32_16x16x32_bf16 v[56:59], v[120:123], a[8:11], 0\n\t"
    "ds_write_b32 v10, v20 offset:1088\n\t"
    "ds_write_b32 v10, v21 offset:1360\n\t"
    "v_mfma_f32_16x16x32_bf16 v[60:63], v[120:123], a[12:15], 0\n\t"
    "ds_write_b32 v10, v22 offset:1632\n\t"
    "ds_write_b32 v10, v23 offset:1904\n\t"
    "s_waitcnt lgkmcnt(8)\n\t"
    "v_mfma_f32_16x16x32_bf16 v[64:67], v[124:127], a[0:3], 0\n\t"
    "buffer_load_dwordx4 v[92:95], v2, s[20:23], s62 offen offset:0 sc1\n\t"
    "ds_write_b32 v10, v24 offset:2176\n\t"
    "ds_write_b32 v10, v25 offset:2448\n\t"
    "v_mfma_f32_16x16x32_bf16 v[68:71], v[124:127], a[4:7], 0\n\t"
    "s_waitcnt lgkmcnt(11)\n\t"
    "L_chk4_%=:\n\t"
    "v_subrev_u32 v4, s49, v4\n\t"
    "v_cmp_gt_u32 vcc, 1, v4\n\t"
    "s_or_b32 s36, vcc_lo, 0x0fffffff\n\t"
    "s_cmp_eq_u32 s36, -1\n\t"
    "s_cbranch_scc0 L_rt4_%=\n\t"
    "L_ok4_%=:\n\t"
    "ds_write_b32 v10, v26 offset:2720\n\t"
    "ds_write_b32 v10, v27 offset:2992\n\t"
    "v_mfma_f32_16x16x32_bf16 v[72:75], v[124:127], a[8:11], 0\n\t"
    "ds_write_b32 v10, v28 offset:3264\n\t"
    "ds_write_b32 v10, v29 offset:3536\n\t"
    "v_mfma_f32_16x16x32_bf16 v[76:79], v[124:127], a[12:15], 0\n\t"
    "ds_write_b32 v10, v30 offset:3808\n\t"
    "ds_write_b32 v10, v31 offset:4080\n\t"
    "s_waitcnt vmcnt(7)\n\t"
    "v_mfma_f32_16x16x32_bf16 v[48:51], v[128:131], a[16:19], v[48:51]\n\t"
    "buffer_load_dwordx4 v[96:99], v2, s[20:23], s59 offen offset:1024 sc1\n\t"
    "ds_write_b32 v10, v32 offset:4352\n\t"
    "ds_write_b32 v10, v33 offset:4624\n\t"
    "v_mfma_f32_16x16x32_bf16 v[52:55], v[128:131], a[20:23], v[52:55]\n\t"
    "ds_write_b32 v10, v34 offset:4896\n\t"
    "ds_write_b32 v10, v35 offset:5168\n\t"
    "v_mfma_f32_16x16x32_bf16 v[56:59], v[128:131], a[24:27], v[56:59]\n\t"
    "ds_write_b32 v10, v36 offset:5440\n\t"
    "ds_write_b32 v10, v37 offset:5712\n\t"
    "v_mfma_f32_16x16x32_bf16 v[60:63], v[128:131], a[28:31], v[60:63]\n\t"
    "ds_write_b32 v10, v38 offset:5984\n\t"
    "ds_write_b32 v10, v39 offset:6256\n\t"
    "s_waitcnt vmcnt(7)\n\t"
    "v_mfma_f32_16x16x32_bf16 v[64:67], v[132:135], a[16:19], v[64:67]\n\t"
    "buffer_load_dwordx4 v[100:103], v2, s[20:23], s60 offen offset:1024 sc1\n\t"
    "ds_write_b32 v10, v40 offset:6528\n\t"
    "ds_write_b32 v10, v41 offset:6800\n\t"
    "v_mfma_f32_16x16x32_bf16 v[68:71], v[132:135], a[20:23], v[68:71]\n\t"
    "ds_write_b32 v10, v42 offset:7072\n\t"
    "ds_write_b32 v10, v43 offset:7344\n\t"
    "v_mfma_f32_16x16x32_bf16 v[72:75], v[132:135], a[24:27], v[72:75]\n\t"
    "ds_write_b32 v10, v44 offset:7616\n\t"
    "ds_write_b32 v10, v45 offset:7888\n\t"
    "v_mfma_f32_16x16x32_bf16 v[76:79], v[132:135], a[28:31], v[76:79]\n\t"
    "ds_write_b32 v10, v46 offset:8160\n\t"
    "ds_write_b32 v10, v47 offset:8432\n\t"
    "v_mov_b32 v12, 3\n\t"
    "ds_write_b32 v11, v12\n\t"
    "s_waitcnt vmcnt(7)\n\t"
    "v_mfma_f32_16x16x32_bf16 v[48:51], v[136:139], a[32:35], v[48:51]\n\t"
    "buffer_load_dwordx4 v[104:107], v2, s[20:23], s61 offen offset:1024 sc1\n\t"
    "v_mfma_f32_16x16x32_bf16 v[52:55], v[136:139], a[36:39], v[52:55]\n\t"
    "v_mfma_f32_16x16x32_bf16 v[56:59], v[136:139], a[40:43], v[56:59]\n\t"
    "v_mfma_f32_16x16x32_bf16 v[60:63], v[136:139], a[44:47], v[60:63]\n\t"
    "s_waitcnt vmcnt(7)\n\t"
    "v_mfma_f32_16x16x32_bf16 v[64:67], v[140:143], a[32:35], v[64:67]\n\t"
    "buffer_load_dwordx4 v[108:111], v2, s[20:23], s62 offen offset:1024 sc1\n\t"
    "v_mfma_f32_16x16x32_bf16 v[68:71], v[140:143], a[36:39], v[68:71]\n\t"
    "v_mfma_f32_16x16x32_bf16 v[72:75], v[140:143], a[40:43], v[72:75]\n\t"
    "v_mfma_f32_16x16x32_bf16 v[76:79], v[140:143], a[44:47], v[76:79]\n\t"
    "s_waitcnt vmcnt(7)\n\t"
    "v_mfma_f32_16x16x32_bf16 v[48:51], v[80:83], a[48:51], v[48:51]\n\t"
    "v_mfma_f32_16x16x32_bf16 v[52:55], v[80:83], a[52:55], v[52:55]\n\t"
    "v_mfma_f32_16x16x32_bf16 v[56:59], v[80:83], a[56:59], v[56:59]\n\t"
    "v_mfma_f32_16x16x32_bf16 v[60:63], v[80:83], a[60:63], v[60:63]\n\t"
    "s_waitcnt vmcnt(6)\n\t"
    "v_mfma_f32_16x16x32_bf16 v[48:51], v[84:87], a[64:67], v[48:51]\n\t"
    "v_mfma_f32_16x16x32_bf16 v[52:55], v[84:87], a[68:71], v[52:55]\n\t"
    "v_mfma_f32_16x16x32_bf16 v[56:59], v[84:87], a[72:75], v[56:59]\n\t"
    "v_mfma_f32_16x16x32_bf16 v[60:63], v[84:87], a[76:79], v[60:63]\n\t"
    "s_waitcnt vmcnt(5)\n\t"
    "v_mfma_f32_16x16x32_bf16 v[48:51], v[88:91], a[80:83], v[48:51]\n\t"
    "v_mfma_f32_16x16x32_bf16 v[52:55], v[88:91], a[84:87], v[52:55]\n\t"
    "v_mfma_f32_16x16x32_bf16 v[56:59], v[88:91], a[88:91], v[56:59]\n\t"
    "v_mfma_f32_16x16x32_bf16 v[60:63], v[88:91], a[92:95], v[60:63]\n\t"
    "s_waitcnt vmcnt(4)\n\t"
    "v_mfma_f32_16x16x32_bf16 v[48:51], v[92:95], a[96:99], v[48:51]\n\t"
    "v_mfma_f32_16x16x32_bf16 v[52:55], v[92:95], a[100:103], v[52:55]\n\t"
    "v_mfma_f32_16x16x32_bf16 v[56:59], v[92:95], a[104:107], v[56:59]\n\t"
    "v_mfma_f32_16x16x32_bf16 v[60:63], v[92:95], a[108:111], v[60:63]\n\t"
    "s_waitcnt vmcnt(3)\n\t"
    "v_mfma_f32_16x16x32_bf16 v[64:67], v[96:99], a[48:51], v[64:67]\n\t"
    "v_mfma_f32_16x16x32_bf16 v[68:71], v[96:99], a[52:55], v[68:71]\n\t"
    "v_mfma_f32_16x16x32_bf16 v[72:75], v[96:99], a[56:59], v[72:75]\n\t"
    "v_mfma_f32_16x16x32_bf16 v[76:79], v[96:99], a[60:63], v[76:79]\n\t"
    "s_waitcnt vmcnt(2)\n\t"
    "v_mfma_f32_16x16x32_bf16 v[64:67], v[100:103], a[64:67], v[64:67]\n\t"
    "ds_write_addtid_b32 v48 offset:0\n\t"
    "ds_write_addtid_b32 v49 offset:272\n\t"
    "v_mfma_f32_16x16x32_bf16 v[68:71], v[100:103], a[68:71], v[68:71]\n\t"
    "ds_write_addtid_b32 v50 offset:544\n\t"
    "ds_write_addtid_b32 v51 offset:816\n\t"
    "v_mfma_f32_16x16x32_bf16 v[72:75], v[100:103], a[72:75], v[72:75]\n\t"
    "ds_write_addtid_b32 v52 offset:1088\n\t"
    "ds_write_addtid_b32 v53 offset:1360\n\t"
    "v_mfma_f32_16x16x32_bf16 v[76:79], v[100:103], a[76:79], v[76:79]\n\t"
    "ds_write_addtid_b32 v54 offset:1632\n\t"
    "ds_write_addtid_b32 v55 offset:1904\n\t"
    "s_waitcnt vmcnt(1)\n\t"
    "v_mfma_f32_16x16x32_bf16 v[64:67], v[104:107], a[80:83], v[64:67]\n\t"
    "ds_write_addtid_b32 v56 offset:2176\n\t"
    "ds_write_addtid_b32 v57 offset:2448\n\t"
    "v_mfma_f32_16x16x32_bf16 v[68:71], v[104:107], a[84:87], v[68:71]\n\t"
    "ds_write_addtid_b32 v58 offset:2720\n\t"
    "ds_write_addtid_b32 v59 offset:2992\n\t"
    "v_mfma_f32_16x16x32_bf16 v[72:75], v[104:107], a[88:91], v[72:75]\n\t"
    "ds_write_addtid_b32 v60 offset:3264\n\t"
    "ds_write_addtid_b32 v61 offset:3536\n\t"
    "v_mfma_f32_16x16x32_bf16 v[76:79], v[104:107], a[92:95], v[76:79]\n\t"
    "ds_write_addtid_b32 v62 offset:3808\n\t"
    "ds_write_addtid_b32 v63 offset:4080\n\t"
    "s_waitcnt vmcnt(0)\n\t"
    "v_mfma_f32_16x16x32_bf16 v[64:67], v[108:111], a[96:99], v[64:67]\n\t"
    "v_mfma_f32_16x16x32_bf16 v[68:71], v[108:111], a[100:103], v[68:71]\n\t"
    "v_mfma_f32_16x16x32_bf16 v[72:75], v[108:111], a[104:107], v[72:75]\n\t"
    "v_mfma_f32_16x16x32_bf16 v[76:79], v[108:111], a[108:111], v[76:79]\n\t"
    "s_nop 4\n\t"
    "ds_write_addtid_b32 v64 offset:4352\n\t"
    "ds_write_addtid_b32 v65 offset:4624\n\t"
    "ds_write_addtid_b32 v66 offset:4896\n\t"
    "ds_write_addtid_b32 v67 offset:5168\n\t"
    "ds_write_addtid_b32 v68 offset:5440\n\t"
    "ds_write_addtid_b32 v69 offset:5712\n\t"
    "ds_write_addtid_b32 v70 offset:5984\n\t"
    "ds_write_addtid_b32 v71 offset:6256\n\t"
    "ds_write_addtid_b32 v72 offset:6528\n\t"
    "ds_write_addtid_b32 v73 offset:6800\n\t"
    "ds_write_addtid_b32 v74 offset:7072\n\t"
    "ds_write_addtid_b32 v75 offset:7344\n\t"
    "ds_write_addtid_b32 v76 offset:7616\n\t"
    "ds_write_addtid_b32 v77 offset:7888\n\t"
    "ds_write_addtid_b32 v78 offset:8160\n\t"
    "ds_write_addtid_b32 v79 offset:8432\n\t"
    "v_mov_b32 v12, 4\n\t"
    "ds_write_b32 v11, v12\n\t"
    "s_waitcnt lgkmcnt(0)\n\t"
    "s_endpgm\n\t"
    "L_c0rt_%=:\n\t"
    "s_add_u32 s40, s40, 32\n\t"
    "s_cmp_gt_u32 s40, 0x400000\n\t"
    "s_cbranch_scc1 L_c0ok_%=\n\t"
    "v_mov_b32 v15, 0\n\t"
    "buffer_atomic_add v15, v3, s[20:23], s32 offen sc0 sc1\n\t"
    "s_waitcnt vmcnt(0)\n\t"
    "s_branch L_c0_%=\n\t"
    "L_rt0_%=:\n\t"
    "s_add_u32 s40, s40, 1\n\t"
    "s_cmp_gt_u32 s40, 0x400000\n\t"
    "s_cbranch_scc1 L_ok0_%=\n\t"
    "ds_read_b32 v4, v14\n\t"
    "s_waitcnt lgkmcnt(0)\n\t"
    "s_branch L_chk0_%=\n\t"
    "L_rt1_%=:\n\t"
    "s_add_u32 s40, s40, 1\n\t"
    "s_cmp_gt_u32 s40, 0x400000\n\t"
    "s_cbranch_scc1 L_ok1_%=\n\t"
    "ds_read_b32 v4, v14\n\t"
    "s_waitcnt lgkmcnt(0)\n\t"
    "s_branch L_chk1_%=\n\t"
    "L_rt2_%=:\n\t"
    "s_add_u32 s40, s40, 1\n\t"
    "s_cmp_gt_u32 s40, 0x400000\n\t"
    "s_cbranch_scc1 L_ok2_%=\n\t"
    "ds_read_b32 v4, v14\n\t"
    "s_waitcnt lgkmcnt(0)\n\t"
    "s_branch L_chk2_%=\n\t"
    "L_rt3_%=:\n\t"
    "s_add_u32 s40, s40, 1\n\t"
    "s_cmp_gt_u32 s40, 0x400000\n\t"
    "s_cbranch_scc1 L_ok3_%=\n\t"
    "ds_read_b32 v4, v14\n\t"
    "s_waitcnt lgkmcnt(0)\n\t"
    "s_branch L_chk3_%=\n\t"
    "L_rt4_%=:\n\t"
    "s_add_u32 s40, s40, 1\n\t"
    "s_cmp_gt_u32 s40, 0x400000\n\t"
    "s_cbranch_scc1 L_ok4_%=\n\t"
    "ds_read_b32 v4, v14\n\t"
    "s_waitcnt lgkmcnt(0)\n\t"
    "s_branch L_chk4_%=\n\t"
    "L_P_%=:\n\t"
    "s_setprio 3\n\t"
    "s_lshl2_add_u32 s29, s36, s35\n\t"
    "v_mov_b32 v16, 0\n\t"
    "v_mov_b32 v17, 0\n\t"
    "v_mov_b32 v18, 0\n\t"
    "v_mov_b32 v19, 0\n\t"
    "v_and_b32 v20, 0xff, v0\n\t"
    "v_lshlrev_b32 v20, 4, v20\n\t"
    "v_add_u32 v20, 159744, v20\n\t"
    "ds_write_b128 v20, v[16:19]\n\t"
    "s_and_b32 s3, s2, 7\n\t"
    "s_lshl_b32 s3, s3, 22\n\t"
    "s_mov_b32 s22, 37748736\n\t"
    "s_mov_b32 s23, 0x20000\n\t"
    "v_lshlrev_b32 v2, 4, v1\n\t"
    "s_lshl_b32 s35, s38, 11\n\t"
    "s_and_b32 s34, s29, 12\n\t"
    "s_mul_i32 s34, s34, 7\n\t"
    "s_add_u32 s34, s34, s29\n\t"
    "s_lshl2_add_u32 s34, s34, 65536\n\t"
    "s_waitcnt lgkmcnt(0)\n\t"
    "s_mov_b32 s20, s64\n\t"
    "s_and_b32 s21, s65, 0xffff\n\t"
    "s_sub_u32 s40, 0, s64\n\t"
    "s_and_b32 s40, s40, 0x1fffff\n\t"
    "s_add_u32 s3, s3, s40\n\t"
    "s_add_u32 s3, s3, 2097152\n\t"
    "buffer_load_dword v4, off, s[20:23], s34 sc1\n\t"
    "s_mov_b32 s24, s62\n\t"
    "s_and_b32 s25, s63, 0xffff\n\t"
    "s_mov_b32 s26, 1835008\n\t"
    "s_mov_b32 s27, 0x20000\n\t"
    "v_bfe_u32 v16, v1, 2, 2\n\t"
    "v_lshrrev_b32 v17, 4, v1\n\t"
    "v_and_b32 v18, 3, v1\n\t"
    "v_lshlrev_b32 v18, 3, v18\n\t"
    "v_and_b32 v19, 16, v1\n\t"
    "v_lshl_add_u32 v18, v19, 1, v18\n\t"
    "v_lshl_add_u32 v19, v17, 3, v16\n\t"
    "v_lshl_add_u32 v19, v19, 8, v18\n\t"
    "v_xor_b32 v16, s38, v16\n\t"
    "v_lshl_add_u32 v8, v16, 6, v19\n\t"
    "v_xor_b32 v9, 32, v8\n\t"
    "v_add_u32 v10, 40960, v8\n\t"
    "v_add_u32 v11, 40960, v9\n\t"
    "v_and_b32 v16, 15, v1\n\t"
    "v_bfe_u32 v18, v1, 1, 3\n\t"
    "v_xor_b32 v18, v18, v17\n\t"
    "v_lshlrev_b32 v18, 4, v18\n\t"
    "v_lshl_add_u32 v12, v16, 7, v18\n\t"
    "v_xor_b32 v13, 64, v12\n\t"
    "v_add_u32 v14, 40960, v12\n\t"
    "v_add_u32 v15, 40960, v13\n\t"
    "v_add_u32 v6, 139264, v12\n\t"
    "v_add_u32 v7, 139264, v13\n\t"
    "v_mov_b32 v16, 0x40000000\n\t"
    "v_cmp_eq_u32 vcc, 0, v1\n\t"
    "s_add_u32 s35, s35, 73728\n\t"
    "s_nop 0\n\t"
    "v_cndmask_b32 v3, v16, v1, vcc\n\t"
    "v_add_u32 v5, s35, v2\n\t"
    "v_lshrrev_b32 v16, 3, v1\n\t"
    "s_lshl_b32 s35, s38, 3\n\t"
    "v_add_u32 v16, s35, v16\n\t"
    "v_and_b32 v17, 7, v1\n\t"
    "v_and_b32 v18, 0x13, v16\n\t"
    "v_lshrrev_b32 v19, 1, v17\n\t"
    "v_lshl_add_u32 v18, v19, 2, v18\n\t"
    "v_mul_u32_u24 v20, 272, v18\n\t"
    "v_and_b32 v18, 12, v16\n\t"
    "v_lshl_add_u32 v20, v18, 4, v20\n\t"
    "v_and_b32 v18, 1, v17\n\t"
    "v_lshl_add_u32 v20, v18, 5, v20\n\t"
    "v_add_u32 v23, 106496, v20\n\t"
    "v_add_u32 v20, 8192, v20\n\t"
    "v_mul_u32_u24 v21, 896, v16\n\t"
    "v_lshl_add_u32 v21, v17, 4, v21\n\t"
    "s_lshl_b32 s47, s29, 9\n\t"
    "s_lshl2_add_u32 s47, s38, s47\n\t"
    "s_lshl_b32 s49, s31, 3\n\t"
    "s_add_u32 s49, s49, s38\n\t"
    "s_lshl_b32 s49, s49, 11\n\t"
    "s_and_b32 s36, s29, 3\n\t"
    "s_lshl_b32 s32, s36, 11\n\t"
    "s_add_u32 s32, s32, s3\n\t"
    "s_add_u32 s32, s32, 1835008\n\t"
    "s_mul_i32 s42, s36, 458752\n\t"
    "s_add_u32 s42, s42, s49\n\t"
    "s_add_u32 s42, s42, s3\n\t"
    "s_mul_i32 s36, s36, 114688\n\t"
    "s_add_u32 s49, s49, s36\n\t"
    "s_add_u32 s49, s49, 2105344\n\t"
    "v_lshlrev_b32 v19, 2, v1\n\t"
    "v_add_u32 v19, 160000, v19\n\t"
    "v_and_b32 v106, 3, v1\n\t"
    "v_cmp_gt_u32 vcc, 5, v1\n\t"
    "v_lshlrev_b32 v106, 2, v106\n\t"
    "v_mov_b32 v16, 0x40000000\n\t"
    "v_cndmask_b32 v106, v16, v106, vcc\n\t"
    "s_lshl1_add_u32 s48, s29, s30\n\t"
    "s_lshl_b32 s48, s48, 8\n\t"
    "s_waitcnt lgkmcnt(0)\n\t"
    "s_barrier\n\t"
    "ds_read_b128 v[64:67], v12 offset:36864\n\t"
    "ds_read_b64_tr_b16 a[0:1], v8 offset:20480\n\t"
    "ds_read_b64_tr_b16 a[2:3], v8 offset:21504\n\t"
    "ds_read_b64_tr_b16 a[4:5], v9 offset:20480\n\t"
    "ds_read_b64_tr_b16 a[6:7], v9 offset:21504\n\t"
    "ds_read_b128 v[68:71], v12 offset:38912\n\t"
    "ds_read_b128 v[72:75], v13 offset:36864\n\t"
    "ds_read_b64_tr_b16 a[8:9], v8 offset:28672\n\t"
    "ds_read_b64_tr_b16 a[10:11], v8 offset:29696\n\t"
    "ds_read_b64_tr_b16 a[12:13], v9 offset:28672\n\t"
    "ds_read_b64_tr_b16 a[14:15], v9 offset:29696\n\t"
    "ds_read_b128 v[76:79], v13 offset:38912\n\t"
    "s_waitcnt lgkmcnt(7)\n\t"
    "v_mfma_f32_16x16x32_bf16 v[24:27], a[0:3], v[64:67], 0\n\t"
    "v_mfma_f32_16x16x32_bf16 v[28:31], a[4:7], v[64:67], 0\n\t"
    "s_waitcnt lgkmcnt(6)\n\t"
    "v_mfma_f32_16x16x32_bf16 v[32:35], a[0:3], v[68:71], 0\n\t"
    "v_mfma_f32_16x16x32_bf16 v[36:39], a[4:7], v[68:71], 0\n\t"
    "s_waitcnt lgkmcnt(1)\n\t"
    "v_mfma_f32_16x16x32_bf16 v[24:27], a[8:11], v[72:75], v[24:27]\n\t"
    "v_mfma_f32_16x16x32_bf16 v[28:31], a[12:15], v[72:75], v[28:31]\n\t"
    "s_waitcnt lgkmcnt(0)\n\t"
    "v_mfma_f32_16x16x32_bf16 v[32:35], a[8:11], v[76:79], v[32:35]\n\t"
    "v_mfma_f32_16x16x32_bf16 v[36:39], a[12:15], v[76:79], v[36:39]\n\t"
    "s_waitcnt lgkmcnt(0)\n\t"
    "s_barrier\n\t"
    "ds_read_b128 v[64:67], v14 offset:16384\n\t"
    "ds_read_b64_tr_b16 a[16:17], v10 offset:0\n\t"
    "ds_read_b64_tr_b16 a[18:19], v10 offset:1024\n\t"
    "ds_read_b64_tr_b16 a[20:21], v11 offset:0\n\t"
    "ds_read_b64_tr_b16 a[22:23], v11 offset:1024\n\t"
    "ds_read_b128 v[68:71], v14 offset:18432\n\t"
    "ds_read_b128 v[72:75], v15 offset:16384\n\t"
    "ds_read_b64_tr_b16 a[24:25], v10 offset:8192\n\t"
    "ds_read_b64_tr_b16 a[26:27], v10 offset:9216\n\t"
    "ds_read_b64_tr_b16 a[28:29], v11 offset:8192\n\t"
    "ds_read_b64_tr_b16 a[30:31], v11 offset:9216\n\t"
    "ds_read_b128 v[76:79], v15 offset:18432\n\t"
    "s_waitcnt lgkmcnt(7)\n\t"
    "v_mfma_f32_16x16x32_bf16 v[24:27], a[16:19], v[64:67], v[24:27]\n\t"
    "v_mfma_f32_16x16x32_bf16 v[28:31], a[20:23], v[64:67], v[28:31]\n\t"
    "s_waitcnt lgkmcnt(6)\n\t"
    "v_mfma_f32_16x16x32_bf16 v[32:35], a[16:19], v[68:71], v[32:35]\n\t"
    "v_mfma_f32_16x16x32_bf16 v[36:39], a[20:23], v[68:71], v[36:39]\n\t"
    "s_waitcnt lgkmcnt(1)\n\t"
    "v_mfma_f32_16x16x32_bf16 v[24:27], a[24:27], v[72:75], v[24:27]\n\t"
    "v_mfma_f32_16x16x32_bf16 v[28:31], a[28:31], v[72:75], v[28:31]\n\t"
    "s_waitcnt lgkmcnt(0)\n\t"
    "v_mfma_f32_16x16x32_bf16 v[32:35], a[24:27], v[76:79], v[32:35]\n\t"
    "v_mfma_f32_16x16x32_bf16 v[36:39], a[28:31], v[76:79], v[36:39]\n\t"
    "s_waitcnt vmcnt(0)\n\t"
    "v_readfirstlane_b32 s39, v4\n\t"
    "s_nop 3\n\t"
    "s_mul_i32 s39, s39, 0x9E3779B1\n\t"
    "s_add_u32 s39, s39, 0x7F4A7C15\n\t"
    "s_bitset0_b32 s39, 31\n\t"
    "s_bitset1_b32 s39, 30\n\t"
    "v_mov_b32 v4, s39\n\t"
    "s_waitcnt lgkmcnt(0)\n\t"
    "s_barrier\n\t"
    "ds_read_b128 v[64:67], v14 offset:36864\n\t"
    "ds_read_b64_tr_b16 a[32:33], v10 offset:20480\n\t"
    "ds_read_b64_tr_b16 a[34:35], v10 offset:21504\n\t"
    "ds_read_b64_tr_b16 a[36:37], v11 offset:20480\n\t"
    "ds_read_b64_tr_b16 a[38:39], v11 offset:21504\n\t"
    "ds_read_b128 v[68:71], v14 offset:38912\n\t"
    "ds_read_b128 v[72:75], v15 offset:36864\n\t"
    "ds_read_b64_tr_b16 a[40:41], v10 offset:28672\n\t"
    "ds_read_b64_tr_b16 a[42:43], v10 offset:29696\n\t"
    "ds_read_b64_tr_b16 a[44:45], v11 offset:28672\n\t"
    "ds_read_b64_tr_b16 a[46:47], v11 offset:29696\n\t"
    "ds_read_b128 v[76:79], v15 offset:38912\n\t"
    "s_waitcnt lgkmcnt(7)\n\t"
    "v_mfma_f32_16x16x32_bf16 v[24:27], a[32:35], v[64:67], v[24:27]\n\t"
    "v_mfma_f32_16x16x32_bf16 v[28:31], a[36:39], v[64:67], v[28:31]\n\t"
    "s_waitcnt lgkmcnt(6)\n\t"
    "v_mfma_f32_16x16x32_bf16 v[32:35], a[32:35], v[68:71], v[32:35]\n\t"
    "v_mfma_f32_16x16x32_bf16 v[36:39], a[36:39], v[68:71], v[36:39]\n\t"
    "s_waitcnt lgkmcnt(1)\n\t"
    "v_mfma_f32_16x16x32_bf16 v[24:27], a[40:43], v[72:75], v[24:27]\n\t"
    "v_mfma_f32_16x16x32_bf16 v[28:31], a[44:47], v[72:75], v[28:31]\n\t"
    "s_waitcnt lgkmcnt(0)\n\t"
    "v_mfma_f32_16x16x32_bf16 v[32:35], a[40:43], v[76:79], v[32:35]\n\t"
    "v_mfma_f32_16x16x32_bf16 v[36:39], a[44:47], v[76:79], v[36:39]\n\t"
    "s_waitcnt lgkmcnt(0)\n\t"
    "s_barrier\n\t"
    "ds_read_b128 v[64:67], v12 offset:16384\n\t"
    "ds_read_b64_tr_b16 a[48:49], v8 offset:0\n\t"
    "ds_read_b64_tr_b16 a[50:51], v8 offset:1024\n\t"
    "ds_read_b64_tr_b16 a[52:53], v9 offset:0\n\t"
    "ds_read_b64_tr_b16 a[54:55], v9 offset:1024\n\t"
    "ds_read_b128 v[68:71], v12 offset:18432\n\t"
    "ds_read_b128 v[72:75], v13 offset:16384\n\t"
    "ds_read_b64_tr_b16 a[56:57], v8 offset:8192\n\t"
    "ds_read_b64_tr_b16 a[58:59], v8 offset:9216\n\t"
    "ds_read_b64_tr_b16 a[60:61], v9 offset:8192\n\t"
    "ds_read_b64_tr_b16 a[62:63], v9 offset:9216\n\t"
    "ds_read_b128 v[76:79], v13 offset:18432\n\t"
    "s_waitcnt lgkmcnt(7)\n\t"
    "v_mfma_f32_16x16x32_bf16 v[24:27], a[48:51], v[64:67], v[24:27]\n\t"
    "v_mfma_f32_16x16x32_bf16 v[28:31], a[52:55], v[64:67], v[28:31]\n\t"
    "s_waitcnt lgkmcnt(6)\n\t"
    "v_mfma_f32_16x16x32_bf16 v[32:35], a[48:51], v[68:71], v[32:35]\n\t"
    "v_mfma_f32_16x16x32_bf16 v[36:39], a[52:55], v[68:71], v[36:39]\n\t"
    "s_waitcnt lgkmcnt(1)\n\t"
    "v_mfma_f32_16x16x32_bf16 v[24:27], a[56:59], v[72:75], v[24:27]\n\t"
    "v_mfma_f32_16x16x32_bf16 v[28:31], a[60:63], v[72:75], v[28:31]\n\t"
    "s_waitcnt lgkmcnt(0)\n\t"
    "v_mfma_f32_16x16x32_bf16 v[32:35], a[56:59], v[76:79], v[32:35]\n\t"
    "v_mfma_f32_16x16x32_bf16 v[36:39], a[60:63], v[76:79], v[36:39]\n\t"
    "s_waitcnt lgkmcnt(0)\n\t"
    "s_barrier\n\t"
    "ds_read_b128 v[64:67], v12 offset:36864\n\t"
    "ds_read_b64_tr_b16 a[64:65], v8 offset:20480\n\t"
    "ds_read_b64_tr_b16 a[66:67], v8 offset:21504\n\t"
    "ds_read_b64_tr_b16 a[68:69], v9 offset:20480\n\t"
    "ds_read_b64_tr_b16 a[70:71], v9 offset:21504\n\t"
    "ds_read_b128 v[68:71], v12 offset:38912\n\t"
    "ds_read_b128 v[72:75], v13 offset:36864\n\t"
    "ds_read_b64_tr_b16 a[72:73], v8 offset:28672\n\t"
    "ds_read_b64_tr_b16 a[74:75], v8 offset:29696\n\t"
    "ds_read_b64_tr_b16 a[76:77], v9 offset:28672\n\t"
    "ds_read_b64_tr_b16 a[78:79], v9 offset:29696\n\t"
    "ds_read_b128 v[76:79], v13 offset:38912\n\t"
    "s_waitcnt lgkmcnt(7)\n\t"
    "v_mfma_f32_16x16x32_bf16 v[24:27], a[64:67], v[64:67], v[24:27]\n\t"
    "v_mfma_f32_16x16x32_bf16 v[28:31], a[68:71], v[64:67], v[28:31]\n\t"
    "s_waitcnt lgkmcnt(6)\n\t"
    "v_mfma_f32_16x16x32_bf16 v[32:35], a[64:67], v[68:71], v[32:35]\n\t"
    "v_mfma_f32_16x16x32_bf16 v[36:39], a[68:71], v[68:71], v[36:39]\n\t"
    "s_waitcnt lgkmcnt(1)\n\t"
    "v_mfma_f32_16x16x32_bf16 v[24:27], a[72:75], v[72:75], v[24:27]\n\t"
    "v_mfma_f32_16x16x32_bf16 v[28:31], a[76:79], v[72:75], v[28:31]\n\t"
    "s_waitcnt lgkmcnt(0)\n\t"
    "v_mfma_f32_16x16x32_bf16 v[32:35], a[72:75], v[76:79], v[32:35]\n\t"
    "v_mfma_f32_16x16x32_bf16 v[36:39], a[76:79], v[76:79], v[36:39]\n\t"
    "s_waitcnt lgkmcnt(0)\n\t"
    "s_barrier\n\t"
    "ds_read_b128 v[64:67], v14 offset:16384\n\t"
    "ds_read_b64_tr_b16 a[80:81], v10 offset:0\n\t"
    "ds_read_b64_tr_b16 a[82:83], v10 offset:1024\n\t"
    "ds_read_b64_tr_b16 a[84:85], v11 offset:0\n\t"
    "ds_read_b64_tr_b16 a[86:87], v11 offset:1024\n\t"
    "ds_read_b128 v[68:71], v14 offset:18432\n\t"
    "ds_read_b128 v[72:75], v15 offset:16384\n\t"
    "ds_read_b64_tr_b16 a[88:89], v10 offset:8192\n\t"
    "ds_read_b64_tr_b16 a[90:91], v10 offset:9216\n\t"
    "ds_read_b64_tr_b16 a[92:93], v11 offset:8192\n\t"
    "ds_read_b64_tr_b16 a[94:95], v11 offset:9216\n\t"
    "ds_read_b128 v[76:79], v15 offset:18432\n\t"
    "s_waitcnt lgkmcnt(7)\n\t"
    "v_mfma_f32_16x16x32_bf16 v[24:27], a[80:83], v[64:67], v[24:27]\n\t"
    "v_mfma_f32_16x16x32_bf16 v[28:31], a[84:87], v[64:67], v[28:31]\n\t"
    "s_waitcnt lgkmcnt(6)\n\t"
    "v_mfma_f32_16x16x32_bf16 v[32:35], a[80:83], v[68:71], v[32:35]\n\t"
    "v_mfma_f32_16x16x32_bf16 v[36:39], a[84:87], v[68:71], v[36:39]\n\t"
    "s_waitcnt lgkmcnt(1)\n\t"
    "v_mfma_f32_16x16x32_bf16 v[24:27], a[88:91], v[72:75], v[24:27]\n\t"
    "v_mfma_f32_16x16x32_bf16 v[28:31], a[92:95], v[72:75], v[28:31]\n\t"
    "s_waitcnt lgkmcnt(0)\n\t"
    "v_mfma_f32_16x16x32_bf16 v[32:35], a[88:91], v[76:79], v[32:35]\n\t"
    "v_mfma_f32_16x16x32_bf16 v[36:39], a[92:95], v[76:79], v[36:39]\n\t"
    "s_waitcnt lgkmcnt(0)\n\t"
    "s_barrier\n\t"
    "ds_read_b32 v19, v19\n\t"
    "ds_read_b128 v[64:67], v14 offset:36864\n\t"
    "ds_read_b64_tr_b16 a[96:97], v10 offset:20480\n\t"
    "ds_read_b64_tr_b16 a[98:99], v10 offset:21504\n\t"
    "ds_read_b64_tr_b16 a[100:101], v11 offset:20480\n\t"
    "ds_read_b64_tr_b16 a[102:103], v11 offset:21504\n\t"
    "ds_read_b128 v[68:71], v14 offset:38912\n\t"
    "ds_read_b128 v[72:75], v15 offset:36864\n\t"
    "ds_read_b64_tr_b16 a[104:105], v10 offset:28672\n\t"
    "ds_read_b64_tr_b16 a[106:107], v10 offset:29696\n\t"
    "ds_read_b64_tr_b16 a[108:109], v11 offset:28672\n\t"
    "ds_read_b64_tr_b16 a[110:111], v11 offset:29696\n\t"
    "ds_read_b128 v[76:79], v15 offset:38912\n\t"
    "s_waitcnt lgkmcnt(7)\n\t"
    "v_mfma_f32_16x16x32_bf16 v[24:27], a[96:99], v[64:67], v[24:27]\n\t"
    "v_mfma_f32_16x16x32_bf16 v[28:31], a[100:103], v[64:67], v[28:31]\n\t"
    "s_waitcnt lgkmcnt(6)\n\t"
    "v_mfma_f32_16x16x32_bf16 v[32:35], a[96:99], v[68:71], v[32:35]\n\t"
    "v_mfma_f32_16x16x32_bf16 v[36:39], a[100:103], v[68:71], v[36:39]\n\t"
    "s_waitcnt lgkmcnt(1)\n\t"
    "v_mfma_f32_16x16x32_bf16 v[24:27], a[104:107], v[72:75], v[24:27]\n\t"
    "v_mfma_f32_16x16x32_bf16 v[28:31], a[108:111], v[72:75], v[28:31]\n\t"
    "s_waitcnt lgkmcnt(0)\n\t"
    "v_mfma_f32_16x16x32_bf16 v[32:35], a[104:107], v[76:79], v[32:35]\n\t"
    "v_mfma_f32_16x16x32_bf16 v[36:39], a[108:111], v[76:79], v[36:39]\n\t"
    "s_mov_b32 s40, 0\n\t"
    "L_pv_%=:\n\t"
    "v_subrev_u32 v19, s39, v19\n\t"
    "v_cmp_gt_u32 vcc, 2, v19\n\t"
    "s_add_u32 s40, s40, 1\n\t"
    "s_cmp_gt_u32 s40, 0x20000\n\t"
    "s_cbranch_scc1 L_pvok_%=\n\t"
    "s_and_b32 s35, vcc_lo, 31\n\t"
    "s_cmp_eq_u32 s35, 31\n\t"
    "s_cbranch_scc1 L_pvok_%=\n\t"
    "buffer_load_dword v19, v106, s[20:23], s48 offen sc1\n\t"
    "s_waitcnt vmcnt(0)\n\t"
    "s_branch L_pv_%=\n\t"
    "L_pvok_%=:\n\t"
    "v_cmp_eq_u32 vcc, 1, v19\n\t"
    "s_and_b32 s35, vcc_lo, 31\n\t"
    "s_bcnt1_i32_b32 s35, s35\n\t"
    "s_cmp_ge_u32 s35, 3\n\t"
    "s_cselect_b32 s35, 1, 0\n\t"
    "s_lshl_b32 s36, s31, 6\n\t"
    "s_add_u32 s36, s36, s38\n\t"
    "s_add_u32 s46, s3, s49\n\t"
    "s_lshl_b32 s35, s35, 13\n\t"
    "s_add_u32 s42, s42, s35\n\t"
    "s_add_u32 s32, s32, s35\n\t"
    "s_sub_u32 s46, s46, s35\n\t"
    "s_lshl2_add_u32 s41, s36, s32\n\t"
    "v_add_u32 v14, 81920, v12\n\t"
    "v_add_u32 v15, 81920, v13\n\t"
    "s_nop 3\n\t"
    "v_cvt_pk_bf16_f32 v56, v24, v25\n\t"
    "v_cvt_pk_bf16_f32 v57, v26, v27\n\t"
    "v_cvt_pk_bf16_f32 v58, v28, v29\n\t"
    "v_cvt_pk_bf16_f32 v59, v30, v31\n\t"
    "v_pk_max_i16 v56, v56, 0\n\t"
    "v_pk_max_i16 v57, v57, 0\n\t"
    "v_pk_max_i16 v58, v58, 0\n\t"
    "v_pk_max_i16 v59, v59, 0\n\t"
    "v_cvt_pk_bf16_f32 v60, v32, v33\n\t"
    "v_cvt_pk_bf16_f32 v61, v34, v35\n\t"
    "v_cvt_pk_bf16_f32 v62, v36, v37\n\t"
    "v_cvt_pk_bf16_f32 v63, v38, v39\n\t"
    "v_pk_max_i16 v60, v60, 0\n\t"
    "v_pk_max_i16 v61, v61, 0\n\t"
    "v_pk_max_i16 v62, v62, 0\n\t"
    "v_pk_max_i16 v63, v63, 0\n\t"
    "buffer_store_dwordx4 v[56:59], v2, s[20:23], s42 offen offset:0 sc1\n\t"
    "buffer_store_dwordx4 v[60:63], v2, s[20:23], s42 offen offset:1024 sc1\n\t"
    "s_waitcnt lgkmcnt(0)\n\t"
    "s_barrier\n\t"
    "ds_read_b128 v[80:83], v14 offset:0\n\t"
    "ds_read_b128 v[84:87], v14 offset:2048\n\t"
    "ds_read_b128 v[88:91], v15 offset:0\n\t"
    "ds_read_b128 v[92:95], v15 offset:2048\n\t"
    "ds_read_b128 v[64:67], v14 offset:4096\n\t"
    "ds_read_b128 v[68:71], v14 offset:6144\n\t"
    "ds_read_b128 v[72:75], v15 offset:4096\n\t"
    "s_waitcnt lgkmcnt(6)\n\t"
    "v_mfma_f32_16x16x32_bf16 v[40:43], a[0:3], v[80:83], 0\n\t"
    "ds_read_b128 v[76:79], v15 offset:6144\n\t"
    "v_mfma_f32_16x16x32_bf16 v[44:47], a[4:7], v[80:83], 0\n\t"
    "s_waitcnt lgkmcnt(6)\n\t"
    "v_mfma_f32_16x16x32_bf16 v[48:51], a[0:3], v[84:87], 0\n\t"
    "ds_read_b128 v[80:83], v14 offset:8192\n\t"
    "v_mfma_f32_16x16x32_bf16 v[52:55], a[4:7], v[84:87], 0\n\t"
    "s_add_u32 s42, s42, 114688\n\t"
    "v_add_u32 v96, 0, v4\n\t"
    "s_waitcnt lgkmcnt(6)\n\t"
    "v_mfma_f32_16x16x32_bf16 v[40:43], a[8:11], v[88:91], v[40:43]\n\t"
    "ds_read_b128 v[84:87], v14 offset:10240\n\t"
    "v_mfma_f32_16x16x32_bf16 v[44:47], a[12:15], v[88:91], v[44:47]\n\t"
    "s_waitcnt lgkmcnt(6)\n\t"
    "v_mfma_f32_16x16x32_bf16 v[48:51], a[8:11], v[92:95], v[48:51]\n\t"
    "ds_read_b128 v[88:91], v15 offset:8192\n\t"
    "v_mfma_f32_16x16x32_bf16 v[52:55], a[12:15], v[92:95], v[52:55]\n\t"
    "s_waitcnt lgkmcnt(6)\n\t"
    "v_mfma_f32_16x16x32_bf16 v[40:43], a[16:19], v[64:67], v[40:43]\n\t"
    "ds_read_b128 v[92:95], v15 offset:10240\n\t"
    "v_mfma_f32_16x16x32_bf16 v[44:47], a[20:23], v[64:67], v[44:47]\n\t"
    "s_waitcnt lgkmcnt(6)\n\t"
    "v_mfma_f32_16x16x32_bf16 v[48:51], a[16:19], v[68:71], v[48:51]\n\t"
    "ds_read_b128 v[64:67], v14 offset:12288\n\t"
    "v_mfma_f32_16x16x32_bf16 v[52:55], a[20:23], v[68:71], v[52:55]\n\t"
    "s_waitcnt lgkmcnt(6)\n\t"
    "v_mfma_f32_16x16x32_bf16 v[40:43], a[24:27], v[72:75], v[40:43]\n\t"
    "ds_read_b128 v[68:71], v14 offset:14336\n\t"
    "v_mfma_f32_16x16x32_bf16 v[44:47], a[28:31], v[72:75], v[44:47]\n\t"
    "s_waitcnt lgkmcnt(6)\n\t"
    "v_mfma_f32_16x16x32_bf16 v[48:51], a[24:27], v[76:79], v[48:51]\n\t"
    "ds_read_b128 v[72:75], v15 offset:12288\n\t"
    "v_mfma_f32_16x16x32_bf16 v[52:55], a[28:31], v[76:79], v[52:55]\n\t"
    "s_waitcnt lgkmcnt(6)\n\t"
    "v_mfma_f32_16x16x32_bf16 v[40:43], a[32:35], v[80:83], v[40:43]\n\t"
    "ds_read_b128 v[76:79], v15 offset:14336\n\t"
    "v_mfma_f32_16x16x32_bf16 v[44:47], a[36:39], v[80:83], v[44:47]\n\t"
    "s_waitcnt lgkmcnt(6)\n\t"
    "v_mfma_f32_16x16x32_bf16 v[48:51], a[32:35], v[84:87], v[48:51]\n\t"
    "ds_read_b128 v[80:83], v14 offset:16384\n\t"
    "v_mfma_f32_16x16x32_bf16 v[52:55], a[36:39], v[84:87], v[52:55]\n\t"
    "s_waitcnt lgkmcnt(6)\n\t"
    "v_mfma_f32_16x16x32_bf16 v[40:43], a[40:43], v[88:91], v[40:43]\n\t"
    "ds_read_b128 v[84:87], v14 offset:18432\n\t"
    "v_mfma_f32_16x16x32_bf16 v[44:47], a[44:47], v[88:91], v[44:47]\n\t"
    "s_waitcnt lgkmcnt(6)\n\t"
    "v_mfma_f32_16x16x32_bf16 v[48:51], a[40:43], v[92:95], v[48:51]\n\t"
    "ds_read_b128 v[88:91], v15 offset:16384\n\t"
    "v_mfma_f32_16x16x32_bf16 v[52:55], a[44:47], v[92:95], v[52:55]\n\t"
    "s_waitcnt lgkmcnt(6)\n\t"
    "v_mfma_f32_16x16x32_bf16 v[40:43], a[48:51], v[64:67], v[40:43]\n\t"
    "ds_read_b128 v[92:95], v15 offset:18432\n\t"
    "v_mfma_f32_16x16x32_bf16 v[44:47], a[52:55], v[64:67], v[44:47]\n\t"
    "s_waitcnt lgkmcnt(6)\n\t"
    "v_mfma_f32_16x16x32_bf16 v[48:51], a[48:51], v[68:71], v[48:51]\n\t"
    "ds_read_b128 v[64:67], v14 offset:20480\n\t"
    "v_mfma_f32_16x16x32_bf16 v[52:55], a[52:55], v[68:71], v[52:55]\n\t"
    "s_waitcnt lgkmcnt(6)\n\t"
    "v_mfma_f32_16x16x32_bf16 v[40:43], a[56:59], v[72:75], v[40:43]\n\t"
    "ds_read_b128 v[68:71], v14 offset:22528\n\t"
    "v_mfma_f32_16x16x32_bf16 v[44:47], a[60:63], v[72:75], v[44:47]\n\t"
    "s_waitcnt lgkmcnt(6)\n\t"
    "v_mfma_f32_16x16x32_bf16 v[48:51], a[56:59], v[76:79], v[48:51]\n\t"
    "ds_read_b128 v[72:75], v15 offset:20480\n\t"
    "v_mfma_f32_16x16x32_bf16 v[52:55], a[60:63], v[76:79], v[52:55]\n\t"
    "s_waitcnt lgkmcnt(6)\n\t"
    "v_mfma_f32_16x16x32_bf16 v[40:43], a[64:67], v[80:83], v[40:43]\n\t"
    "ds_read_b128 v[76:79], v15 offset:22528\n\t"
    "v_mfma_f32_16x16x32_bf16 v[44:47], a[68:71], v[80:83], v[44:47]\n\t"
    "s_waitcnt lgkmcnt(6)\n\t"
    "v_mfma_f32_16x16x32_bf16 v[48:51], a[64:67], v[84:87], v[48:51]\n\t"
    "ds_read_b128 v[80:83], v14 offset:24576\n\t"
    "v_mfma_f32_16x16x32_bf16 v[52:55], a[68:71], v[84:87], v[52:55]\n\t"
    "s_waitcnt lgkmcnt(6)\n\t"
    "v_mfma_f32_16x16x32_bf16 v[40:43], a[72:75], v[88:91], v[40:43]\n\t"
    "ds_read_b128 v[84:87], v14 offset:26624\n\t"
    "v_mfma_f32_16x16x32_bf16 v[44:47], a[76:79], v[88:91], v[44:47]\n\t"
    "s_waitcnt lgkmcnt(6)\n\t"
    "v_mfma_f32_16x16x32_bf16 v[48:51], a[72:75], v[92:95], v[48:51]\n\t"
    "ds_read_b128 v[88:91], v15 offset:24576\n\t"
    "v_mfma_f32_16x16x32_bf16 v[52:55], a[76:79], v[92:95], v[52:55]\n\t"
    "s_waitcnt lgkmcnt(6)\n\t"
    "v_mfma_f32_16x16x32_bf16 v[40:43], a[80:83], v[64:67], v[40:43]\n\t"
    "ds_read_b128 v[92:95], v15 offset:26624\n\t"
    "v_mfma_f32_16x16x32_bf16 v[44:47], a[84:87], v[64:67], v[44:47]\n\t"
    "s_waitcnt lgkmcnt(6)\n\t"
    "v_mfma_f32_16x16x32_bf16 v[48:51], a[80:83], v[68:71], v[48:51]\n\t"
    "v_mfma_f32_16x16x32_bf16 v[52:55], a[84:87], v[68:71], v[52:55]\n\t"
    "s_waitcnt lgkmcnt(5)\n\t"
    "v_mfma_f32_16x16x32_bf16 v[40:43], a[88:91], v[72:75], v[40:43]\n\t"
    "v_mfma_f32_16x16x32_bf16 v[44:47], a[92:95], v[72:75], v[44:47]\n\t"
    "s_waitcnt lgkmcnt(4)\n\t"
    "v_mfma_f32_16x16x32_bf16 v[48:51], a[88:91], v[76:79], v[48:51]\n\t"
    "v_mfma_f32_16x16x32_bf16 v[52:55], a[92:95], v[76:79], v[52:55]\n\t"
    "s_waitcnt lgkmcnt(3)\n\t"
    "v_mfma_f32_16x16x32_bf16 v[40:43], a[96:99], v[80:83], v[40:43]\n\t"
    "v_mfma_f32_16x16x32_bf16 v[44:47], a[100:103], v[80:83], v[44:47]\n\t"
    "s_waitcnt vmcnt(0)\n\t"
    "buffer_atomic_swap v96, v3, s[20:23], s41 offen sc1\n\t"
    "s_waitcnt lgkmcnt(2)\n\t"
    "v_mfma_f32_16x16x32_bf16 v[48:51], a[96:99], v[84:87], v[48:51]\n\t"
    "v_mfma_f32_16x16x32_bf16 v[52:55], a[100:103], v[84:87], v[52:55]\n\t"
    "s_waitcnt lgkmcnt(1)\n\t"
    "v_mfma_f32_16x16x32_bf16 v[40:43], a[104:107], v[88:91], v[40:43]\n\t"
    "v_mfma_f32_16x16x32_bf16 v[44:47], a[108:111], v[88:91], v[44:47]\n\t"
    "s_waitcnt lgkmcnt(0)\n\t"
    "v_mfma_f32_16x16x32_bf16 v[48:51], a[104:107], v[92:95], v[48:51]\n\t"
    "v_mfma_f32_16x16x32_bf16 v[52:55], a[108:111], v[92:95], v[52:55]\n\t"
    "s_waitcnt lgkmcnt(0)\n\t"
    "s_barrier\n\t"
    "ds_read_b128 v[64:67], v14 offset:28672\n\t"
    "ds_read_b128 v[68:71], v14 offset:30720\n\t"
    "ds_read_b128 v[72:75], v15 offset:28672\n\t"
    "ds_read_b128 v[76:79], v15 offset:30720\n\t"
    "ds_read_b128 v[80:83], v14 offset:32768\n\t"
    "ds_read_b128 v[84:87], v14 offset:34816\n\t"
    "ds_read_b128 v[88:91], v15 offset:32768\n\t"
    "s_waitcnt lgkmcnt(6)\n\t"
    "v_mfma_f32_16x16x32_bf16 v[24:27], a[0:3], v[64:67], 0\n\t"
    "ds_read_b128 v[92:95], v15 offset:34816\n\t"
    "v_mfma_f32_16x16x32_bf16 v[28:31], a[4:7], v[64:67], 0\n\t"
    "s_waitcnt lgkmcnt(6)\n\t"
    "v_mfma_f32_16x16x32_bf16 v[32:35], a[0:3], v[68:71], 0\n\t"
    "ds_read_b128 v[64:67], v14 offset:36864\n\t"
    "v_mfma_f32_16x16x32_bf16 v[36:39], a[4:7], v[68:71], 0\n\t"
    "v_cvt_pk_bf16_f32 v108, v40, v41\n\t"
    "v_cvt_pk_bf16_f32 v109, v42, v43\n\t"
    "s_waitcnt lgkmcnt(6)\n\t"
    "v_mfma_f32_16x16x32_bf16 v[24:27], a[8:11], v[72:75], v[24:27]\n\t"
    "ds_read_b128 v[68:71], v14 offset:38912\n\t"
    "v_mfma_f32_16x16x32_bf16 v[28:31], a[12:15], v[72:75], v[28:31]\n\t"
    "v_cvt_pk_bf16_f32 v110, v44, v45\n\t"
    "v_cvt_pk_bf16_f32 v111, v46, v47\n\t"
    "s_waitcnt lgkmcnt(6)\n\t"
    "v_mfma_f32_16x16x32_bf16 v[32:35], a[8:11], v[76:79], v[32:35]\n\t"
    "ds_read_b128 v[72:75], v15 offset:36864\n\t"
    "v_mfma_f32_16x16x32_bf16 v[36:39], a[12:15], v[76:79], v[36:39]\n\t"
    "v_pk_max_i16 v108, v108, 0\n\t"
    "v_pk_max_i16 v109, v109, 0\n\t"
    "s_waitcnt lgkmcnt(6)\n\t"
    "v_mfma_f32_16x16x32_bf16 v[24:27], a[16:19], v[80:83], v[24:27]\n\t"
    "ds_read_b128 v[76:79], v15 offset:38912\n\t"
    "v_mfma_f32_16x16x32_bf16 v[28:31], a[20:23], v[80:83], v[28:31]\n\t"
    "v_pk_max_i16 v110, v110, 0\n\t"
    "v_pk_max_i16 v111, v111, 0\n\t"
    "s_waitcnt lgkmcnt(6)\n\t"
    "v_mfma_f32_16x16x32_bf16 v[32:35], a[16:19], v[84:87], v[32:35]\n\t"
    "ds_read_b128 v[80:83], v14 offset:40960\n\t"
    "v_mfma_f32_16x16x32_bf16 v[36:39], a[20:23], v[84:87], v[36:39]\n\t"
    "v_cvt_pk_bf16_f32 v112, v48, v49\n\t"
    "v_cvt_pk_bf16_f32 v113, v50, v51\n\t"
    "s_waitcnt lgkmcnt(6)\n\t"
    "v_mfma_f32_16x16x32_bf16 v[24:27], a[24:27], v[88:91], v[24:27]\n\t"
    "ds_read_b128 v[84:87], v14 offset:43008\n\t"
    "v_mfma_f32_16x16x32_bf16 v[28:31], a[28:31], v[88:91], v[28:31]\n\t"
    "v_cvt_pk_bf16_f32 v114, v52, v53\n\t"
    "v_cvt_pk_bf16_f32 v115, v54, v55\n\t"
    "s_waitcnt lgkmcnt(6)\n\t"
    "v_mfma_f32_16x16x32_bf16 v[32:35], a[24:27], v[92:95], v[32:35]\n\t"
    "ds_read_b128 v[88:91], v15 offset:40960\n\t"
    "v_mfma_f32_16x16x32_bf16 v[36:39], a[28:31], v[92:95], v[36:39]\n\t"
    "v_pk_max_i16 v112, v112, 0\n\t"
    "v_pk_max_i16 v113, v113, 0\n\t"
    "s_waitcnt lgkmcnt(6)\n\t"
    "v_mfma_f32_16x16x32_bf16 v[24:27], a[32:35], v[64:67], v[24:27]\n\t"
    "ds_read_b128 v[92:95], v15 offset:43008\n\t"
    "v_mfma_f32_16x16x32_bf16 v[28:31], a[36:39], v[64:67], v[28:31]\n\t"
    "v_pk_max_i16 v114, v114, 0\n\t"
    "v_pk_max_i16 v115, v115, 0\n\t"
    "s_waitcnt lgkmcnt(6)\n\t"
    "v_mfma_f32_16x16x32_bf16 v[32:35], a[32:35], v[68:71], v[32:35]\n\t"
    "ds_read_b128 v[64:67], v14 offset:45056\n\t"
    "v_mfma_f32_16x16x32_bf16 v[36:39], a[36:39], v[68:71], v[36:39]\n\t"
    "buffer_store_dwordx4 v[108:111], v2, s[20:23], s42 offen offset:0 sc1\n\t"
    "buffer_store_dwordx4 v[112:115], v2, s[20:23], s42 offen offset:1024 sc1\n\t"
    "s_add_u32 s42, s42, 114688\n\t"
    "s_waitcnt lgkmcnt(6)\n\t"
    "v_mfma_f32_16x16x32_bf16 v[24:27], a[40:43], v[72:75], v[24:27]\n\t"
    "ds_read_b128 v[68:71], v14 offset:47104\n\t"
    "v_mfma_f32_16x16x32_bf16 v[28:31], a[44:47], v[72:75], v[28:31]\n\t"
    "v_add_u32 v97, 1, v4\n\t"
    "s_waitcnt lgkmcnt(6)\n\t"
    "v_mfma_f32_16x16x32_bf16 v[32:35], a[40:43], v[76:79], v[32:35]\n\t"
    "ds_read_b128 v[72:75], v15 offset:45056\n\t"
    "v_mfma_f32_16x16x32_bf16 v[36:39], a[44:47], v[76:79], v[36:39]\n\t"
    "s_waitcnt lgkmcnt(6)\n\t"
    "v_mfma_f32_16x16x32_bf16 v[24:27], a[48:51], v[80:83], v[24:27]\n\t"
    "ds_read_b128 v[76:79], v15 offset:47104\n\t"
    "v_mfma_f32_16x16x32_bf16 v[28:31], a[52:55], v[80:83], v[28:31]\n\t"
    "s_waitcnt lgkmcnt(6)\n\t"
    "v_mfma_f32_16x16x32_bf16 v[32:35], a[48:51], v[84:87], v[32:35]\n\t"
    "ds_read_b128 v[80:83], v14 offset:49152\n\t"
    "v_mfma_f32_16x16x32_bf16 v[36:39], a[52:55], v[84:87], v[36:39]\n\t"
    "s_waitcnt lgkmcnt(6)\n\t"
    "v_mfma_f32_16x16x32_bf16 v[24:27], a[56:59], v[88:91], v[24:27]\n\t"
    "ds_read_b128 v[84:87], v14 offset:51200\n\t"
    "v_mfma_f32_16x16x32_bf16 v[28:31], a[60:63], v[88:91], v[28:31]\n\t"
    "s_waitcnt lgkmcnt(6)\n\t"
    "v_mfma_f32_16x16x32_bf16 v[32:35], a[56:59], v[92:95], v[32:35]\n\t"
    "ds_read_b128 v[88:91], v15 offset:49152\n\t"
    "v_mfma_f32_16x16x32_bf16 v[36:39], a[60:63], v[92:95], v[36:39]\n\t"
    "s_waitcnt lgkmcnt(6)\n\t"
    "v_mfma_f32_16x16x32_bf16 v[24:27], a[64:67], v[64:67], v[24:27]\n\t"
    "ds_read_b128 v[92:95], v15 offset:51200\n\t"
    "v_mfma_f32_16x16x32_bf16 v[28:31], a[68:71], v[64:67], v[28:31]\n\t"
    "s_waitcnt lgkmcnt(6)\n\t"
    "v_mfma_f32_16x16x32_bf16 v[32:35], a[64:67], v[68:71], v[32:35]\n\t"
    "ds_read_b128 v[64:67], v14 offset:53248\n\t"
    "v_mfma_f32_16x16x32_bf16 v[36:39], a[68:71], v[68:71], v[36:39]\n\t"
    "s_waitcnt lgkmcnt(6)\n\t"
    "v_mfma_f32_16x16x32_bf16 v[24:27], a[72:75], v[72:75], v[24:27]\n\t"
    "ds_read_b128 v[68:71], v14 offset:55296\n\t"
    "v_mfma_f32_16x16x32_bf16 v[28:31], a[76:79], v[72:75], v[28:31]\n\t"
    "s_waitcnt lgkmcnt(6)\n\t"
    "v_mfma_f32_16x16x32_bf16 v[32:35], a[72:75], v[76:79], v[32:35]\n\t"
    "ds_read_b128 v[72:75], v15 offset:53248\n\t"
    "v_mfma_f32_16x16x32_bf16 v[36:39], a[76:79], v[76:79], v[36:39]\n\t"
    "s_waitcnt lgkmcnt(6)\n\t"
    "v_mfma_f32_16x16x32_bf16 v[24:27], a[80:83], v[80:83], v[24:27]\n\t"
    "ds_read_b128 v[76:79], v15 offset:55296\n\t"
    "v_mfma_f32_16x16x32_bf16 v[28:31], a[84:87], v[80:83], v[28:31]\n\t"
    "s_waitcnt lgkmcnt(6)\n\t"
    "v_mfma_f32_16x16x32_bf16 v[32:35], a[80:83], v[84:87], v[32:35]\n\t"
    "ds_read_b128 v[80:83], v6 offset:0\n\t"
    "v_mfma_f32_16x16x32_bf16 v[36:39], a[84:87], v[84:87], v[36:39]\n\t"
    "s_waitcnt lgkmcnt(6)\n\t"
    "v_mfma_f32_16x16x32_bf16 v[24:27], a[88:91], v[88:91], v[24:27]\n\t"
    "ds_read_b128 v[84:87], v6 offset:2048\n\t"
    "v_mfma_f32_16x16x32_bf16 v[28:31], a[92:95], v[88:91], v[28:31]\n\t"
    "s_waitcnt lgkmcnt(6)\n\t"
    "v_mfma_f32_16x16x32_bf16 v[32:35], a[88:91], v[92:95], v[32:35]\n\t"
    "ds_read_b128 v[88:91], v7 offset:0\n\t"
    "v_mfma_f32_16x16x32_bf16 v[36:39], a[92:95], v[92:95], v[36:39]\n\t"
    "s_waitcnt lgkmcnt(6)\n\t"
    "v_mfma_f32_16x16x32_bf16 v[24:27], a[96:99], v[64:67], v[24:27]\n\t"
    "ds_read_b128 v[92:95], v7 offset:2048\n\t"
    "v_mfma_f32_16x16x32_bf16 v[28:31], a[100:103], v[64:67], v[28:31]\n\t"
    "s_waitcnt lgkmcnt(6)\n\t"
    "v_mfma_f32_16x16x32_bf16 v[32:35], a[96:99], v[68:71], v[32:35]\n\t"
    "ds_read_b128 v[64:67], v6 offset:4096\n\t"
    "v_mfma_f32_16x16x32_bf16 v[36:39], a[100:103], v[68:71], v[36:39]\n\t"
    "s_waitcnt lgkmcnt(6)\n\t"
    "v_mfma_f32_16x16x32_bf16 v[24:27], a[104:107], v[72:75], v[24:27]\n\t"
    "ds_read_b128 v[68:71], v6 offset:6144\n\t"
    "v_mfma_f32_16x16x32_bf16 v[28:31], a[108:111], v[72:75], v[28:31]\n\t"
    "s_waitcnt lgkmcnt(6)\n\t"
    "v_mfma_f32_16x16x32_bf16 v[32:35], a[104:107], v[76:79], v[32:35]\n\t"
    "ds_read_b128 v[72:75], v7 offset:4096\n\t"
    "v_mfma_f32_16x16x32_bf16 v[36:39], a[108:111], v[76:79], v[36:39]\n\t"
    "s_waitcnt vmcnt(0)\n\t"
    "buffer_atomic_swap v97, v3, s[20:23], s41 offen sc1\n\t"
    "s_waitcnt lgkmcnt(6)\n\t"
    "v_mfma_f32_16x16x32_bf16 v[40:43], a[0:3], v[80:83], 0\n\t"
    "ds_read_b128 v[76:79], v7 offset:6144\n\t"
    "v_mfma_f32_16x16x32_bf16 v[44:47], a[4:7], v[80:83], 0\n\t"
    "s_waitcnt lgkmcnt(6)\n\t"
    "v_mfma_f32_16x16x32_bf16 v[48:51], a[0:3], v[84:87], 0\n\t"
    "ds_read_b128 v[80:83], v6 offset:8192\n\t"
    "v_mfma_f32_16x16x32_bf16 v[52:55], a[4:7], v[84:87], 0\n\t"
    "s_waitcnt lgkmcnt(6)\n\t"
    "v_mfma_f32_16x16x32_bf16 v[40:43], a[8:11], v[88:91], v[40:43]\n\t"
    "ds_read_b128 v[84:87], v6 offset:10240\n\t"
    "v_mfma_f32_16x16x32_bf16 v[44:47], a[12:15], v[88:91], v[44:47]\n\t"
    "s_waitcnt lgkmcnt(6)\n\t"
    "v_mfma_f32_16x16x32_bf16 v[48:51], a[8:11], v[92:95], v[48:51]\n\t"
    "ds_read_b128 v[88:91], v7 offset:8192\n\t"
    "v_mfma_f32_16x16x32_bf16 v[52:55], a[12:15], v[92:95], v[52:55]\n\t"
    "s_waitcnt lgkmcnt(6)\n\t"
    "v_mfma_f32_16x16x32_bf16 v[40:43], a[16:19], v[64:67], v[40:43]\n\t"
    "ds_read_b128 v[92:95], v7 offset:10240\n\t"
    "v_mfma_f32_16x16x32_bf16 v[44:47], a[20:23], v[64:67], v[44:47]\n\t"
    "v_cvt_pk_bf16_f32 v116, v24, v25\n\t"
    "v_cvt_pk_bf16_f32 v117, v26, v27\n\t"
    "s_waitcnt lgkmcnt(6)\n\t"
    "v_mfma_f32_16x16x32_bf16 v[48:51], a[16:19], v[68:71], v[48:51]\n\t"
    "ds_read_b128 v[64:67], v6 offset:12288\n\t"
    "v_mfma_f32_16x16x32_bf16 v[52:55], a[20:23], v[68:71], v[52:55]\n\t"
    "v_cvt_pk_bf16_f32 v118, v28, v29\n\t"
    "v_cvt_pk_bf16_f32 v119, v30, v31\n\t"
    "s_waitcnt lgkmcnt(6)\n\t"
    "v_mfma_f32_16x16x32_bf16 v[40:43], a[24:27], v[72:75], v[40:43]\n\t"
    "ds_read_b128 v[68:71], v6 offset:14336\n\t"
    "v_mfma_f32_16x16x32_bf16 v[44:47], a[28:31], v[72:75], v[44:47]\n\t"
    "v_pk_max_i16 v116, v116, 0\n\t"
    "v_pk_max_i16 v117, v117, 0\n\t"
    "s_waitcnt lgkmcnt(6)\n\t"
    "v_mfma_f32_16x16x32_bf16 v[48:51], a[24:27], v[76:79], v[48:51]\n\t"
    "ds_read_b128 v[72:75], v7 offset:12288\n\t"
    "v_mfma_f32_16x16x32_bf16 v[52:55], a[28:31], v[76:79], v[52:55]\n\t"
    "v_pk_max_i16 v118, v118, 0\n\t"
    "v_pk_max_i16 v119, v119, 0\n\t"
    "s_waitcnt lgkmcnt(6)\n\t"
    "v_mfma_f32_16x16x32_bf16 v[40:43], a[32:35], v[80:83], v[40:43]\n\t"
    "ds_read_b128 v[76:79], v7 offset:14336\n\t"
    "v_mfma_f32_16x16x32_bf16 v[44:47], a[36:39], v[80:83], v[44:47]\n\t"
    "v_cvt_pk_bf16_f32 v120, v32, v33\n\t"
    "v_cvt_pk_bf16_f32 v121, v34, v35\n\t"
    "s_waitcnt lgkmcnt(6)\n\t"
    "v_mfma_f32_16x16x32_bf16 v[48:51], a[32:35], v[84:87], v[48:51]\n\t"
    "ds_read_b128 v[80:83], v6 offset:16384\n\t"
    "v_mfma_f32_16x16x32_bf16 v[52:55], a[36:39], v[84:87], v[52:55]\n\t"
    "v_cvt_pk_bf16_f32 v122, v36, v37\n\t"
    "v_cvt_pk_bf16_f32 v123, v38, v39\n\t"
    "s_waitcnt lgkmcnt(6)\n\t"
    "v_mfma_f32_16x16x32_bf16 v[40:43], a[40:43], v[88:91], v[40:43]\n\t"
    "ds_read_b128 v[84:87], v6 offset:18432\n\t"
    "v_mfma_f32_16x16x32_bf16 v[44:47], a[44:47], v[88:91], v[44:47]\n\t"
    "v_pk_max_i16 v120, v120, 0\n\t"
    "v_pk_max_i16 v121, v121, 0\n\t"
    "s_waitcnt lgkmcnt(6)\n\t"
    "v_mfma_f32_16x16x32_bf16 v[48:51], a[40:43], v[92:95], v[48:51]\n\t"
    "ds_read_b128 v[88:91], v7 offset:16384\n\t"
    "v_mfma_f32_16x16x32_bf16 v[52:55], a[44:47], v[92:95], v[52:55]\n\t"
    "v_pk_max_i16 v122, v122, 0\n\t"
    "v_pk_max_i16 v123, v123, 0\n\t"
    "s_waitcnt lgkmcnt(6)\n\t"
    "v_mfma_f32_16x16x32_bf16 v[40:43], a[48:51], v[64:67], v[40:43]\n\t"
    "ds_read_b128 v[92:95], v7 offset:18432\n\t"
    "v_mfma_f32_16x16x32_bf16 v[44:47], a[52:55], v[64:67], v[44:47]\n\t"
    "buffer_store_dwordx4 v[116:119], v2, s[20:23], s42 offen offset:0 sc1\n\t"
    "buffer_store_dwordx4 v[120:123], v2, s[20:23], s42 offen offset:1024 sc1\n\t"
    "s_add_u32 s42, s42, 114688\n\t"
    "s_waitcnt lgkmcnt(6)\n\t"
    "v_mfma_f32_16x16x32_bf16 v[48:51], a[48:51], v[68:71], v[48:51]\n\t"
    "ds_read_b128 v[64:67], v12 offset:0\n\t"
    "v_mfma_f32_16x16x32_bf16 v[52:55], a[52:55], v[68:71], v[52:55]\n\t"
    "v_add_u32 v98, 2, v4\n\t"
    "s_waitcnt lgkmcnt(6)\n\t"
    "v_mfma_f32_16x16x32_bf16 v[40:43], a[56:59], v[72:75], v[40:43]\n\t"
    "ds_read_b128 v[68:71], v12 offset:2048\n\t"
    "v_mfma_f32_16x16x32_bf16 v[44:47], a[60:63], v[72:75], v[44:47]\n\t"
    "s_waitcnt lgkmcnt(6)\n\t"
    "v_mfma_f32_16x16x32_bf16 v[48:51], a[56:59], v[76:79], v[48:51]\n\t"
    "ds_read_b128 v[72:75], v13 offset:0\n\t"
    "v_mfma_f32_16x16x32_bf16 v[52:55], a[60:63], v[76:79], v[52:55]\n\t"
    "s_waitcnt lgkmcnt(6)\n\t"
    "v_mfma_f32_16x16x32_bf16 v[40:43], a[64:67], v[80:83], v[40:43]\n\t"
    "ds_read_b128 v[76:79], v13 offset:2048\n\t"
    "v_mfma_f32_16x16x32_bf16 v[44:47], a[68:71], v[80:83], v[44:47]\n\t"
    "s_waitcnt lgkmcnt(6)\n\t"
    "v_mfma_f32_16x16x32_bf16 v[48:51], a[64:67], v[84:87], v[48:51]\n\t"
    "ds_read_b128 v[80:83], v12 offset:4096\n\t"
    "v_mfma_f32_16x16x32_bf16 v[52:55], a[68:71], v[84:87], v[52:55]\n\t"
    "s_waitcnt lgkmcnt(6)\n\t"
    "v_mfma_f32_16x16x32_bf16 v[40:43], a[72:75], v[88:91], v[40:43]\n\t"
    "ds_read_b128 v[84:87], v12 offset:6144\n\t"
    "v_mfma_f32_16x16x32_bf16 v[44:47], a[76:79], v[88:91], v[44:47]\n\t"
    "s_waitcnt lgkmcnt(6)\n\t"
    "v_mfma_f32_16x16x32_bf16 v[48:51], a[72:75], v[92:95], v[48:51]\n\t"
    "ds_read_b128 v[88:91], v13 offset:4096\n\t"
    "v_mfma_f32_16x16x32_bf16 v[52:55], a[76:79], v[92:95], v[52:55]\n\t"
    "s_waitcnt lgkmcnt(6)\n\t"
    "v_mfma_f32_16x16x32_bf16 v[40:43], a[80:83], v[64:67], v[40:43]\n\t"
    "ds_read_b128 v[92:95], v13 offset:6144\n\t"
    "v_mfma_f32_16x16x32_bf16 v[44:47], a[84:87], v[64:67], v[44:47]\n\t"
    "s_waitcnt lgkmcnt(6)\n\t"
    "v_mfma_f32_16x16x32_bf16 v[48:51], a[80:83], v[68:71], v[48:51]\n\t"
    "v_mfma_f32_16x16x32_bf16 v[52:55], a[84:87], v[68:71], v[52:55]\n\t"
    "s_waitcnt lgkmcnt(5)\n\t"
    "v_mfma_f32_16x16x32_bf16 v[40:43], a[88:91], v[72:75], v[40:43]\n\t"
    "v_mfma_f32_16x16x32_bf16 v[44:47], a[92:95], v[72:75], v[44:47]\n\t"
    "s_waitcnt lgkmcnt(4)\n\t"
    "v_mfma_f32_16x16x32_bf16 v[48:51], a[88:91], v[76:79], v[48:51]\n\t"
    "v_mfma_f32_16x16x32_bf16 v[52:55], a[92:95], v[76:79], v[52:55]\n\t"
    "s_waitcnt lgkmcnt(3)\n\t"
    "v_mfma_f32_16x16x32_bf16 v[40:43], a[96:99], v[80:83], v[40:43]\n\t"
    "v_mfma_f32_16x16x32_bf16 v[44:47], a[100:103], v[80:83], v[44:47]\n\t"
    "s_waitcnt lgkmcnt(2)\n\t"
    "v_mfma_f32_16x16x32_bf16 v[48:51], a[96:99], v[84:87], v[48:51]\n\t"
    "v_mfma_f32_16x16x32_bf16 v[52:55], a[100:103], v[84:87], v[52:55]\n\t"
    "s_waitcnt lgkmcnt(1)\n\t"
    "v_mfma_f32_16x16x32_bf16 v[40:43], a[104:107], v[88:91], v[40:43]\n\t"
    "v_mfma_f32_16x16x32_bf16 v[44:47], a[108:111], v[88:91], v[44:47]\n\t"
    "s_waitcnt lgkmcnt(0)\n\t"
    "v_mfma_f32_16x16x32_bf16 v[48:51], a[104:107], v[92:95], v[48:51]\n\t"
    "v_mfma_f32_16x16x32_bf16 v[52:55], a[108:111], v[92:95], v[52:55]\n\t"
    "s_waitcnt vmcnt(0)\n\t"
    "buffer_atomic_swap v98, v3, s[20:23], s41 offen sc1\n\t"
    "s_nop 7\n\t"
    "s_nop 3\n\t"
    "v_cvt_pk_bf16_f32 v56, v40, v41\n\t"
    "v_cvt_pk_bf16_f32 v57, v42, v43\n\t"
    "v_cvt_pk_bf16_f32 v58, v44, v45\n\t"
    "v_cvt_pk_bf16_f32 v59, v46, v47\n\t"
    "v_pk_max_i16 v56, v56, 0\n\t"
    "v_pk_max_i16 v57, v57, 0\n\t"
    "v_pk_max_i16 v58, v58, 0\n\t"
    "v_pk_max_i16 v59, v59, 0\n\t"
    "v_cvt_pk_bf16_f32 v60, v48, v49\n\t"
    "v_cvt_pk_bf16_f32 v61, v50, v51\n\t"
    "v_cvt_pk_bf16_f32 v62, v52, v53\n\t"
    "v_cvt_pk_bf16_f32 v63, v54, v55\n\t"
    "v_pk_max_i16 v60, v60, 0\n\t"
    "v_pk_max_i16 v61, v61, 0\n\t"
    "v_pk_max_i16 v62, v62, 0\n\t"
    "v_pk_max_i16 v63, v63, 0\n\t"
    "buffer_store_dwordx4 v[56:59], v2, s[20:23], s42 offen offset:0 sc1\n\t"
    "buffer_store_dwordx4 v[60:63], v2, s[20:23], s42 offen offset:1024 sc1\n\t"
    "ds_write_b128 v5, v[108:111] offset:8192\n\t"
    "ds_write_b128 v5, v[112:115] offset:9216\n\t"
    "ds_write_b128 v5, v[116:119] offset:16384\n\t"
    "ds_write_b128 v5, v[120:123] offset:17408\n\t"
    "ds_write_b128 v5, v[56:59] offset:24576\n\t"
    "ds_write_b128 v5, v[60:63] offset:25600\n\t"
    "s_add_u32 s42, s42, 114688\n\t"
    "v_add_u32 v99, 3, v4\n\t"
    "s_lshl_b32 s35, s38, 8\n\t"
    "s_add_u32 s35, s35, 160768\n\t"
    "v_lshl_add_u32 v130, v1, 2, s35\n\t"
    "ds_write_b32 v130, v98\n\t"
    "v_and_b32 v131, 3, v1\n\t"
    "v_lshlrev_b32 v131, 8, v131\n\t"
    "v_add_u32 v131, 161792, v131\n\t"
    "v_add_u32 v22, 3, v4\n\t"
    "v_mov_b32 v16, 0x40000000\n\t"
    "v_lshlrev_b32 v17, 8, v1\n\t"
    "v_cmp_gt_u32 vcc, 2, v1\n\t"
    "s_nop 1\n\t"
    "v_cndmask_b32 v129, v16, v17, vcc\n\t"
    "s_waitcnt vmcnt(0)\n\t"
    "buffer_atomic_swap v99, v3, s[20:23], s41 offen sc1\n\t"
    "s_lshl_b32 s35, s29, 1\n\t"
    "s_xor_b32 s36, s30, 1\n\t"
    "s_add_u32 s35, s35, s36\n\t"
    "s_mul_i32 s35, s35, 57344\n\t"
    "s_lshl_b32 s36, s31, 7\n\t"
    "s_add_u32 s35, s35, s36\n\t"
    "s_lshl_b32 s36, s30, 23\n\t"
    "s_sub_u32 s34, s3, s36\n\t"
    "s_add_u32 s34, s34, s49\n\t"
    "s_add_u32 s34, s34, 4194304\n\t"
    "s_cmp_lg_u32 s38, 0\n\t"
    "s_cbranch_scc1 L_nop0_%=\n\t"
    "v_mov_b32 v16, 0x40000000\n\t"
    "v_lshlrev_b32 v17, 2, v1\n\t"
    "v_cmp_gt_u32 vcc, 28, v1\n\t"
    "v_add_u32 v18, 160512, v17\n\t"
    "v_and_b32 v19, 3, v1\n\t"
    "v_lshlrev_b32 v19, 2, v19\n\t"
    "v_lshrrev_b32 v17, 2, v1\n\t"
    "v_lshl_add_u32 v17, v17, 8, v19\n\t"
    "s_mov_b32 s40, 0\n\t"
    "v_cndmask_b32 v17, v16, v17, vcc\n\t"
    "L_p0p_%=:\n\t"
    "v_mov_b32 v16, 0\n\t"
    "buffer_atomic_add v16, v17, s[20:23], s32 offen sc0 sc1\n\t"
    "s_waitcnt vmcnt(0)\n\t"
    "ds_write_b32 v18, v16\n\t"
    "v_cmp_eq_u32 vcc, v22, v16\n\t"
    "s_add_u32 s40, s40, 1\n\t"
    "s_cmp_gt_u32 s40, 0x20000\n\t"
    "s_cbranch_scc1 L_nop0_%=\n\t"
    "s_or_b32 s37, vcc_lo, 0xf0000000\n\t"
    "s_cmp_eq_u32 s37, -1\n\t"
    "s_cbranch_scc0 L_p0p_%=\n\t"
    "L_nop0_%=:\n\t"
    "s_setprio 0\n\t"
    "s_mov_b32 s40, 0\n\t"
    "L_dn0_%=:\n\t"
    "ds_read_b32 v16, v131\n\t"
    "s_add_u32 s40, s40, 1\n\t"
    "s_cmp_gt_u32 s40, 0x400000\n\t"
    "s_cbranch_scc1 L_dnok0_%=\n\t"
    "s_sleep 1\n\t"
    "s_waitcnt lgkmcnt(0)\n\t"
    "v_cmp_lt_u32 vcc, 0, v16\n\t"
    "s_cmp_eq_u32 vcc_lo, -1\n\t"
    "s_cbranch_scc0 L_dn0_%=\n\t"
    "L_dnok0_%=:\n\t"
    "ds_read_b128 v[96:99], v20 offset:0\n\t"
    "ds_read_b128 v[100:103], v20 offset:16\n\t"
    "ds_read_b128 v[104:107], v20 offset:18432\n\t"
    "ds_read_b128 v[108:111], v20 offset:18448\n\t"
    "ds_read_b128 v[112:115], v20 offset:36864\n\t"
    "ds_read_b128 v[116:119], v20 offset:36880\n\t"
    "ds_read_b128 v[120:123], v20 offset:55296\n\t"
    "ds_read_b128 v[124:127], v20 offset:55312\n\t"
    "ds_write_b32 v130, v22\n\t"
    "s_waitcnt lgkmcnt(5)\n\t"
    "v_add_f32 v96, v96, v104\n\t"
    "v_add_f32 v97, v97, v105\n\t"
    "v_add_f32 v98, v98, v106\n\t"
    "v_add_f32 v99, v99, v107\n\t"
    "v_add_f32 v100, v100, v108\n\t"
    "v_add_f32 v101, v101, v109\n\t"
    "v_add_f32 v102, v102, v110\n\t"
    "v_add_f32 v103, v103, v111\n\t"
    "s_waitcnt lgkmcnt(3)\n\t"
    "v_add_f32 v96, v96, v112\n\t"
    "v_add_f32 v97, v97, v113\n\t"
    "v_add_f32 v98, v98, v114\n\t"
    "v_add_f32 v99, v99, v115\n\t"
    "v_add_f32 v100, v100, v116\n\t"
    "v_add_f32 v101, v101, v117\n\t"
    "v_add_f32 v102, v102, v118\n\t"
    "v_add_f32 v103, v103, v119\n\t"
    "s_waitcnt lgkmcnt(1)\n\t"
    "v_add_f32 v96, v96, v120\n\t"
    "v_add_f32 v97, v97, v121\n\t"
    "v_add_f32 v98, v98, v122\n\t"
    "v_add_f32 v99, v99, v123\n\t"
    "v_add_f32 v100, v100, v124\n\t"
    "v_add_f32 v101, v101, v125\n\t"
    "v_add_f32 v102, v102, v126\n\t"
    "v_add_f32 v103, v103, v127\n\t"
    "v_cvt_pk_bf16_f32 v136, v96, v97\n\t"
    "v_cvt_pk_bf16_f32 v137, v98, v99\n\t"
    "v_cvt_pk_bf16_f32 v138, v100, v101\n\t"
    "v_cvt_pk_bf16_f32 v139, v102, v103\n\t"
    "v_min_u32 v136, -2, v136\n\t"
    "v_min_u32 v137, -2, v137\n\t"
    "v_min_u32 v138, -2, v138\n\t"
    "v_min_u32 v139, -2, v139\n\t"
    "v_mov_b32 v128, 0\n\t"
    "buffer_load_dword v128, v129, s[20:23], s47 offen offset:128 sc1\n\t"
    "s_waitcnt vmcnt(0)\n\t"
    "s_mov_b32 s40, 0\n\t"
    "L_ib_%=:\n\t"
    "v_subrev_u32 v128, s39, v128\n\t"
    "v_cmp_gt_u32 vcc, 2, v128\n\t"
    "s_add_u32 s40, s40, 1\n\t"
    "s_cmp_gt_u32 s40, 0x20000\n\t"
    "s_cbranch_scc1 L_ibok_%=\n\t"
    "s_and_b32 s36, vcc_lo, 3\n\t"
    "s_cmp_eq_u32 s36, 3\n\t"
    "s_cbranch_scc1 L_ibok_%=\n\t"
    "s_sleep 2\n\t"
    "v_mov_b32 v128, 0\n\t"
    "buffer_load_dword v128, v129, s[20:23], s47 offen offset:128 sc1\n\t"
    "s_waitcnt vmcnt(0)\n\t"
    "s_branch L_ib_%=\n\t"
    "L_ibok_%=:\n\t"
    "s_xor_b32 s36, s30, 1\n\t"
    "v_readlane_b32 s36, v128, s36\n\t"
    "s_nop 3\n\t"
    "s_and_b32 s36, s36, 1\n\t"
    "s_lshl_b32 s36, s36, 13\n\t"
    "s_sub_u32 s34, s34, s36\n\t"
    "s_mov_b32 s40, 0\n\t"
    "L_dn1_%=:\n\t"
    "ds_read_b32 v16, v131\n\t"
    "s_add_u32 s40, s40, 1\n\t"
    "s_cmp_gt_u32 s40, 0x400000\n\t"
    "s_cbranch_scc1 L_dnok1_%=\n\t"
    "s_sleep 1\n\t"
    "s_waitcnt lgkmcnt(0)\n\t"
    "v_cmp_lt_u32 vcc, 1, v16\n\t"
    "s_cmp_eq_u32 vcc_lo, -1\n\t"
    "s_cbranch_scc0 L_dn1_%=\n\t"
    "L_dnok1_%=:\n\t"
    "ds_read_b128 v[96:99], v20 offset:8704\n\t"
    "ds_read_b128 v[100:103], v20 offset:8720\n\t"
    "ds_read_b128 v[104:107], v20 offset:27136\n\t"
    "ds_read_b128 v[108:111], v20 offset:27152\n\t"
    "ds_read_b128 v[112:115], v20 offset:45568\n\t"
    "ds_read_b128 v[116:119], v20 offset:45584\n\t"
    "ds_read_b128 v[120:123], v20 offset:64000\n\t"
    "ds_read_b128 v[124:127], v20 offset:64016\n\t"
    "s_waitcnt lgkmcnt(4)\n\t"
    "v_add_f32 v96, v96, v104\n\t"
    "v_add_f32 v97, v97, v105\n\t"
    "v_add_f32 v98, v98, v106\n\t"
    "v_add_f32 v99, v99, v107\n\t"
    "v_add_f32 v100, v100, v108\n\t"
    "v_add_f32 v101, v101, v109\n\t"
    "v_add_f32 v102, v102, v110\n\t"
    "v_add_f32 v103, v103, v111\n\t"
    "s_waitcnt lgkmcnt(2)\n\t"
    "v_add_f32 v96, v96, v112\n\t"
    "v_add_f32 v97, v97, v113\n\t"
    "v_add_f32 v98, v98, v114\n\t"
    "v_add_f32 v99, v99, v115\n\t"
    "v_add_f32 v100, v100, v116\n\t"
    "v_add_f32 v101, v101, v117\n\t"
    "v_add_f32 v102, v102, v118\n\t"
    "v_add_f32 v103, v103, v119\n\t"
    "s_waitcnt lgkmcnt(0)\n\t"
    "v_add_f32 v96, v96, v120\n\t"
    "v_add_f32 v97, v97, v121\n\t"
    "v_add_f32 v98, v98, v122\n\t"
    "v_add_f32 v99, v99, v123\n\t"
    "v_add_f32 v100, v100, v124\n\t"
    "v_add_f32 v101, v101, v125\n\t"
    "v_add_f32 v102, v102, v126\n\t"
    "v_add_f32 v103, v103, v127\n\t"
    "v_cvt_pk_bf16_f32 v140, v96, v97\n\t"
    "v_cvt_pk_bf16_f32 v141, v98, v99\n\t"
    "v_cvt_pk_bf16_f32 v142, v100, v101\n\t"
    "v_cvt_pk_bf16_f32 v143, v102, v103\n\t"
    "v_min_u32 v140, -2, v140\n\t"
    "v_min_u32 v141, -2, v141\n\t"
    "v_min_u32 v142, -2, v142\n\t"
    "v_min_u32 v143, -2, v143\n\t"
    "buffer_store_dwordx4 v[136:139], v2, s[20:23], s34 offen offset:0 sc1\n\t"
    "buffer_store_dwordx4 v[140:143], v2, s[20:23], s34 offen offset:1024 sc1\n\t"
    "s_mov_b32 s40, 0\n\t"
    "L_dn2_%=:\n\t"
    "ds_read_b32 v16, v131\n\t"
    "s_add_u32 s40, s40, 1\n\t"
    "s_cmp_gt_u32 s40, 0x400000\n\t"
    "s_cbranch_scc1 L_dnok2_%=\n\t"
    "s_sleep 1\n\t"
    "s_waitcnt lgkmcnt(0)\n\t"
    "v_cmp_lt_u32 vcc, 2, v16\n\t"
    "s_cmp_eq_u32 vcc_lo, -1\n\t"
    "s_cbranch_scc0 L_dn2_%=\n\t"
    "L_dnok2_%=:\n\t"
    "buffer_load_dwordx4 v[24:27], v2, s[20:23], s46 offen offset:0 sc1\n\t"
    "buffer_load_dwordx4 v[28:31], v2, s[20:23], s46 offen offset:1024 sc1\n\t"
    "ds_read_b128 v[96:99], v23 offset:0\n\t"
    "ds_read_b128 v[100:103], v23 offset:16\n\t"
    "ds_read_b128 v[104:107], v23 offset:8704\n\t"
    "ds_read_b128 v[108:111], v23 offset:8720\n\t"
    "ds_read_b128 v[112:115], v23 offset:17408\n\t"
    "ds_read_b128 v[116:119], v23 offset:17424\n\t"
    "ds_read_b128 v[120:123], v23 offset:26112\n\t"
    "ds_read_b128 v[124:127], v23 offset:26128\n\t"
    "s_waitcnt lgkmcnt(4)\n\t"
    "v_add_f32 v96, v96, v104\n\t"
    "v_add_f32 v97, v97, v105\n\t"
    "v_add_f32 v98, v98, v106\n\t"
    "v_add_f32 v99, v99, v107\n\t"
    "v_add_f32 v100, v100, v108\n\t"
    "v_add_f32 v101, v101, v109\n\t"
    "v_add_f32 v102, v102, v110\n\t"
    "v_add_f32 v103, v103, v111\n\t"
    "s_waitcnt lgkmcnt(2)\n\t"
    "v_add_f32 v96, v96, v112\n\t"
    "v_add_f32 v97, v97, v113\n\t"
    "v_add_f32 v98, v98, v114\n\t"
    "v_add_f32 v99, v99, v115\n\t"
    "v_add_f32 v100, v100, v116\n\t"
    "v_add_f32 v101, v101, v117\n\t"
    "v_add_f32 v102, v102, v118\n\t"
    "v_add_f32 v103, v103, v119\n\t"
    "s_waitcnt lgkmcnt(0)\n\t"
    "v_add_f32 v80, v96, v120\n\t"
    "v_add_f32 v81, v97, v121\n\t"
    "v_add_f32 v82, v98, v122\n\t"
    "v_add_f32 v83, v99, v123\n\t"
    "v_add_f32 v84, v100, v124\n\t"
    "v_add_f32 v85, v101, v125\n\t"
    "v_add_f32 v86, v102, v126\n\t"
    "v_add_f32 v87, v103, v127\n\t"
    "s_waitcnt vmcnt(0)\n\t"
    "s_mov_b32 s40, 0\n\t"
    "L_pr_chk_%=:\n\t"
    "v_max3_u32 v16, v24, v25, v26\n\t"
    "v_max3_u32 v17, v27, v28, v29\n\t"
    "v_max3_u32 v16, v16, v30, v31\n\t"
    "v_max_u32 v16, v16, v17\n\t"
    "v_cmp_eq_u32 vcc, -1, v16\n\t"
    "s_cmp_eq_u64 vcc, 0\n\t"
    "s_cbranch_scc1 L_pr_ok_%=\n\t"
    "s_add_u32 s40, s40, 1\n\t"
    "s_cmp_gt_u32 s40, 0x20000\n\t"
    "s_cbranch_scc1 L_pr_ok_%=\n\t"
    "buffer_load_dwordx4 v[24:27], v2, s[20:23], s46 offen offset:0 sc1\n\t"
    "buffer_load_dwordx4 v[28:31], v2, s[20:23], s46 offen offset:1024 sc1\n\t"
    "s_waitcnt vmcnt(0)\n\t"
    "s_branch L_pr_chk_%=\n\t"
    "L_pr_ok_%=:\n\t"
    "v_lshlrev_b32 v64, 16, v24\n\t"
    "v_and_b32 v65, 0xffff0000, v24\n\t"
    "v_lshlrev_b32 v66, 16, v25\n\t"
    "v_and_b32 v67, 0xffff0000, v25\n\t"
    "v_lshlrev_b32 v68, 16, v26\n\t"
    "v_and_b32 v69, 0xffff0000, v26\n\t"
    "v_lshlrev_b32 v70, 16, v27\n\t"
    "v_and_b32 v71, 0xffff0000, v27\n\t"
    "v_lshlrev_b32 v72, 16, v28\n\t"
    "v_and_b32 v73, 0xffff0000, v28\n\t"
    "v_lshlrev_b32 v74, 16, v29\n\t"
    "v_and_b32 v75, 0xffff0000, v29\n\t"
    "v_lshlrev_b32 v76, 16, v30\n\t"
    "v_and_b32 v77, 0xffff0000, v30\n\t"
    "v_lshlrev_b32 v78, 16, v31\n\t"
    "v_and_b32 v79, 0xffff0000, v31\n\t"
    "v_add_f32 v80, v80, v64\n\t"
    "v_add_f32 v81, v81, v65\n\t"
    "v_add_f32 v82, v82, v66\n\t"
    "v_add_f32 v83, v83, v67\n\t"
    "v_add_f32 v84, v84, v68\n\t"
    "v_add_f32 v85, v85, v69\n\t"
    "v_add_f32 v86, v86, v70\n\t"
    "v_add_f32 v87, v87, v71\n\t"
    "v_cvt_pk_bf16_f32 v136, v80, v81\n\t"
    "v_cvt_pk_bf16_f32 v137, v82, v83\n\t"
    "v_cvt_pk_bf16_f32 v138, v84, v85\n\t"
    "v_cvt_pk_bf16_f32 v139, v86, v87\n\t"
    "buffer_store_dwordx4 v[136:139], v21, s[24:27], s35 offen sc0 sc1\n\t"
    "s_add_u32 s35, s35, 28672\n\t"
    "s_mov_b32 s40, 0\n\t"
    "L_dn3_%=:\n\t"
    "ds_read_b32 v16, v131\n\t"
    "s_add_u32 s40, s40, 1\n\t"
    "s_cmp_gt_u32 s40, 0x400000\n\t"
    "s_cbranch_scc1 L_dnok3_%=\n\t"
    "s_waitcnt lgkmcnt(0)\n\t"
    "v_cmp_lt_u32 vcc, 3, v16\n\t"
    "s_cmp_eq_u32 vcc_lo, -1\n\t"
    "s_cbranch_scc0 L_dn3_%=\n\t"
    "L_dnok3_%=:\n\t"
    "ds_read_b128 v[96:99], v20 offset:0\n\t"
    "ds_read_b128 v[100:103], v20 offset:16\n\t"
    "ds_read_b128 v[104:107], v20 offset:18432\n\t"
    "ds_read_b128 v[108:111], v20 offset:18448\n\t"
    "ds_read_b128 v[112:115], v20 offset:36864\n\t"
    "ds_read_b128 v[116:119], v20 offset:36880\n\t"
    "ds_read_b128 v[120:123], v20 offset:55296\n\t"
    "ds_read_b128 v[124:127], v20 offset:55312\n\t"
    "s_waitcnt lgkmcnt(6)\n\t"
    "v_add_f32 v96, v96, v72\n\t"
    "v_add_f32 v97, v97, v73\n\t"
    "v_add_f32 v98, v98, v74\n\t"
    "v_add_f32 v99, v99, v75\n\t"
    "v_add_f32 v100, v100, v76\n\t"
    "v_add_f32 v101, v101, v77\n\t"
    "v_add_f32 v102, v102, v78\n\t"
    "v_add_f32 v103, v103, v79\n\t"
    "s_waitcnt lgkmcnt(4)\n\t"
    "v_add_f32 v96, v96, v104\n\t"
    "v_add_f32 v97, v97, v105\n\t"
    "v_add_f32 v98, v98, v106\n\t"
    "v_add_f32 v99, v99, v107\n\t"
    "v_add_f32 v100, v100, v108\n\t"
    "v_add_f32 v101, v101, v109\n\t"
    "v_add_f32 v102, v102, v110\n\t"
    "v_add_f32 v103, v103, v111\n\t"
    "s_waitcnt lgkmcnt(2)\n\t"
    "v_add_f32 v96, v96, v112\n\t"
    "v_add_f32 v97, v97, v113\n\t"
    "v_add_f32 v98, v98, v114\n\t"
    "v_add_f32 v99, v99, v115\n\t"
    "v_add_f32 v100, v100, v116\n\t"
    "v_add_f32 v101, v101, v117\n\t"
    "v_add_f32 v102, v102, v118\n\t"
    "v_add_f32 v103, v103, v119\n\t"
    "s_waitcnt lgkmcnt(0)\n\t"
    "v_add_f32 v96, v96, v120\n\t"
    "v_add_f32 v97, v97, v121\n\t"
    "v_add_f32 v98, v98, v122\n\t"
    "v_add_f32 v99, v99, v123\n\t"
    "v_add_f32 v100, v100, v124\n\t"
    "v_add_f32 v101, v101, v125\n\t"
    "v_add_f32 v102, v102, v126\n\t"
    "v_add_f32 v103, v103, v127\n\t"
    "v_cvt_pk_bf16_f32 v140, v96, v97\n\t"
    "v_cvt_pk_bf16_f32 v141, v98, v99\n\t"
    "v_cvt_pk_bf16_f32 v142, v100, v101\n\t"
    "v_cvt_pk_bf16_f32 v143, v102, v103\n\t"
    "buffer_store_dwordx4 v[140:143], v21, s[24:27], s35 offen sc0 sc1\n\t"
    "s_endpgm\n\t"
    "L_Hquit_%=:\n\t"
    "s_endpgm\n\t"
    "L_H_%=:\n\t"
    "v_lshlrev_b32 v3, 7, v1\n\t"
    "v_min_u32 v3, 7040, v3\n\t"
    "s_lshl2_add_u32 s29, s36, s31\n\t"
    "s_mul_i32 s35, s29, 114688\n\t"
    "s_mov_b32 m0, 0\n\t"
    "s_mov_b32 s10, 1835008\n\t"
    "s_mov_b32 s11, 0x20000\n\t"
    "s_mov_b32 s8, s56\n\t"
    "s_and_b32 s9, s57, 0xffff\n\t"
    "s_and_b32 s3, s2, 7\n\t"
    "s_lshl_b32 s3, s3, 22\n\t"
    "s_sub_u32 s40, 0, s64\n\t"
    "s_and_b32 s40, s40, 0x1fffff\n\t"
    "s_add_u32 s3, s3, s40\n\t"
    "s_add_u32 s3, s3, 2097152\n\t"
    "s_mov_b32 s18, 1605632\n\t"
    "s_mov_b32 s19, 0x20000\n\t"
    "s_mov_b32 s16, s60\n\t"
    "s_and_b32 s17, s61, 0xffff\n\t"
    "s_mul_i32 s45, s36, 7\n\t"
    "s_add_u32 s45, s45, s31\n\t"
    "s_mul_i32 s33, s30, 802816\n\t"
    "s_lshl1_add_u32 s46, s29, s30\n\t"
    "s_lshl_b32 s46, s46, 8\n\t"
    "s_cmp_lg_u32 s43, 7\n\t"
    "s_cselect_b32 s43, 1, 0\n\t"
    "s_mov_b32 s37, 0x9E3779B1\n\t"
    "s_waitcnt vmcnt(5)\n\t"
    "v_mul_lo_u32 v7, v7, s37\n\t"
    "v_add_u32 v7, 0x7F4A7C15, v7\n\t"
    "v_and_b32 v7, 0x3fffffff, v7\n\t"
    "v_or_b32 v7, 0x40000000, v7\n\t"
    "v_add_u32 v8, s43, v7\n\t"
    "buffer_atomic_swap v8, v5, s[20:23], s41 offen sc1\n\t"
    "v_readlane_b32 s39, v7, s31\n\t"
    "s_lshl_b32 s37, s31, 11\n\t"
    "s_add_u32 s37, s37, s3\n\t"
    "s_add_u32 s42, s37, 1835008\n\t"
    "v_and_b32 v9, 1, v1\n\t"
    "v_lshlrev_b32 v9, 13, v9\n\t"
    "v_bfe_u32 v10, v1, 1, 3\n\t"
    "v_min_u32 v10, 6, v10\n\t"
    "v_lshl_add_u32 v9, v10, 8, v9\n\t"
    "buffer_load_dword v9, v9, s[20:23], s42 offen sc1\n\t"
    "buffer_load_dword v10, off, s[20:23], s46 offset:128 sc1\n\t"
    "s_lshl1_add_u32 s37, s30, 1\n\t"
    "s_and_b32 s37, s37, 3\n\t"
    "s_mul_i32 s37, s37, 28672\n\t"
    "s_add_u32 s37, s37, s35\n\t"
    "buffer_load_dword v3, s[8:11], s37 offen lds\n\t"
    "s_add_u32 s37, s37, 7168\n\t"
    "buffer_load_dword v3, s[8:11], s37 offen lds\n\t"
    "s_add_u32 s37, s37, 7168\n\t"
    "buffer_load_dword v3, s[8:11], s37 offen lds\n\t"
    "s_add_u32 s37, s37, 7168\n\t"
    "buffer_load_dword v3, s[8:11], s37 offen lds\n\t"
    "s_add_u32 s37, s45, 0\n\t"
    "s_cmp_ge_u32 s37, 28\n\t"
    "s_cselect_b32 s34, 28, 0\n\t"
    "s_sub_u32 s37, s37, s34\n\t"
    "s_mul_i32 s37, s37, 28672\n\t"
    "s_add_u32 s37, s37, s33\n\t"
    "buffer_load_dword v3, s[16:19], s37 offen lds\n\t"
    "s_add_u32 s37, s37, 7168\n\t"
    "buffer_load_dword v3, s[16:19], s37 offen lds\n\t"
    "s_add_u32 s37, s37, 7168\n\t"
    "buffer_load_dword v3, s[16:19], s37 offen lds\n\t"
    "s_add_u32 s37, s37, 7168\n\t"
    "buffer_load_dword v3, s[16:19], s37 offen lds\n\t"
    "v_and_b32 v12, 3, v1\n\t"
    "v_cmp_gt_u32 vcc, 5, v1\n\t"
    "v_lshlrev_b32 v12, 2, v12\n\t"
    "v_mov_b32 v13, 0x40000000\n\t"
    "v_cndmask_b32 v12, v13, v12, vcc\n\t"
    "buffer_load_dword v13, v12, s[20:23], s46 offen sc1\n\t"
    "s_add_u32 s37, s45, 4\n\t"
    "s_cmp_ge_u32 s37, 28\n\t"
    "s_cselect_b32 s34, 28, 0\n\t"
    "s_sub_u32 s37, s37, s34\n\t"
    "s_mul_i32 s37, s37, 28672\n\t"
    "s_add_u32 s37, s37, s33\n\t"
    "buffer_load_dword v3, s[16:19], s37 offen lds\n\t"
    "s_add_u32 s37, s37, 7168\n\t"
    "buffer_load_dword v3, s[16:19], s37 offen lds\n\t"
    "s_add_u32 s37, s37, 7168\n\t"
    "buffer_load_dword v3, s[16:19], s37 offen lds\n\t"
    "s_add_u32 s37, s37, 7168\n\t"
    "buffer_load_dword v3, s[16:19], s37 offen lds\n\t"
    "v_lshlrev_b32 v14, 4, v1\n\t"
    "v_mov_b32 v16, -1\n\t"
    "v_mov_b32 v17, -1\n\t"
    "v_mov_b32 v18, -1\n\t"
    "v_mov_b32 v19, -1\n\t"
    "s_waitcnt vmcnt(4)\n\t"
    "s_mov_b32 s40, 0\n\t"
    "L_hv_%=:\n\t"
    "v_subrev_u32 v13, s39, v13\n\t"
    "v_cmp_gt_u32 vcc, 2, v13\n\t"
    "s_add_u32 s40, s40, 1\n\t"
    "s_cmp_gt_u32 s40, 0x20000\n\t"
    "s_cbranch_scc1 L_hvok_%=\n\t"
    "s_and_b32 s44, vcc_lo, 31\n\t"
    "s_cmp_eq_u32 s44, 31\n\t"
    "s_cbranch_scc1 L_hvok_%=\n\t"
    "buffer_load_dword v13, v12, s[20:23], s46 offen sc1\n\t"
    "s_waitcnt vmcnt(0)\n\t"
    "s_branch L_hv_%=\n\t"
    "L_hvok_%=:\n\t"
    "v_cmp_eq_u32 vcc, 1, v13\n\t"
    "s_and_b32 s44, vcc_lo, 31\n\t"
    "s_bcnt1_i32_b32 s44, s44\n\t"
    "s_cmp_ge_u32 s44, 3\n\t"
    "s_cselect_b32 s44, 1, 0\n\t"
    "s_lshl_b32 s34, s44, 13\n\t"
    "s_mul_i32 s37, s31, 114688\n\t"
    "s_add_u32 s37, s37, s3\n\t"
    "s_sub_u32 s37, s37, s34\n\t"
    "s_add_u32 s37, s37, 2105344\n\t"
    "s_mov_b32 s40, 7\n\t"
    "L_pz_%=:\n\t"
    "buffer_store_dwordx4 v[16:19], v14, s[20:23], s37 offen offset:0 sc1\n\t"
    "buffer_store_dwordx4 v[16:19], v14, s[20:23], s37 offen offset:1024 sc1\n\t"
    "buffer_store_dwordx4 v[16:19], v14, s[20:23], s37 offen offset:2048 sc1\n\t"
    "buffer_store_dwordx4 v[16:19], v14, s[20:23], s37 offen offset:3072 sc1\n\t"
    "s_add_u32 s37, s37, 4096\n\t"
    "buffer_store_dwordx4 v[16:19], v14, s[20:23], s37 offen offset:0 sc1\n\t"
    "buffer_store_dwordx4 v[16:19], v14, s[20:23], s37 offen offset:1024 sc1\n\t"
    "buffer_store_dwordx4 v[16:19], v14, s[20:23], s37 offen offset:2048 sc1\n\t"
    "buffer_store_dwordx4 v[16:19], v14, s[20:23], s37 offen offset:3072 sc1\n\t"
    "s_add_u32 s37, s37, 12288\n\t"
    "s_sub_u32 s40, s40, 1\n\t"
    "s_cmp_lg_u32 s40, 0\n\t"
    "s_cbranch_scc1 L_pz_%=\n\t"
    "v_and_b32 v14, 31, v1\n\t"
    "v_lshlrev_b32 v14, 2, v14\n\t"
    "s_add_u32 s44, s44, s39\n\t"
    "v_mov_b32 v15, s44\n\t"
    "s_waitcnt vmcnt(0)\n\t"
    "buffer_store_dword v15, v14, s[20:23], s46 offen offset:128 sc1\n\t"
    "s_cmp_lg_u32 s30, 0\n\t"
    "s_cbranch_scc1 L_hend_%=\n\t"
    "v_and_b32 v10, 32, v1\n\t"
    "v_lshl_add_u32 v8, v10, 3, v14\n\t"
    "s_add_u32 s37, s46, 128\n\t"
    "s_mov_b32 s40, 0\n\t"
    "L_he_%=:\n\t"
    "v_mov_b32 v9, 0\n\t"
    "buffer_atomic_add v9, v8, s[20:23], s37 offen sc0 sc1\n\t"
    "s_add_u32 s40, s40, 1\n\t"
    "s_cmp_gt_u32 s40, 0x20000\n\t"
    "s_cbranch_scc1 L_hes_%=\n\t"
    "s_waitcnt vmcnt(0)\n\t"
    "v_subrev_u32 v9, s39, v9\n\t"
    "v_cmp_gt_u32 vcc, 2, v9\n\t"
    "s_cmp_eq_u64 vcc, -1\n\t"
    "s_cbranch_scc1 L_heok_%=\n\t"
    "s_sleep 8\n\t"
    "s_branch L_he_%=\n\t"
    "L_heok_%=:\n\t"
    "v_and_b32 v11, 31, v1\n\t"
    "v_min_u32 v11, 27, v11\n\t"
    "v_and_b32 v12, 3, v11\n\t"
    "v_lshlrev_b32 v12, 2, v12\n\t"
    "v_lshrrev_b32 v8, 2, v11\n\t"
    "v_lshl_add_u32 v8, v8, 8, v12\n\t"
    "v_lshl_add_u32 v8, v10, 17, v8\n\t"
    "v_lshl_add_u32 v8, v9, 13, v8\n\t"
    "L_hf_%=:\n\t"
    "v_mov_b32 v9, 0\n\t"
    "buffer_atomic_add v9, v8, s[20:23], s42 offen sc0 sc1\n\t"
    "s_add_u32 s40, s40, 1\n\t"
    "s_cmp_gt_u32 s40, 0x20000\n\t"
    "s_cbranch_scc1 L_hes_%=\n\t"
    "s_waitcnt vmcnt(0)\n\t"
    "v_subrev_u32 v9, s39, v9\n\t"
    "v_cmp_gt_u32 vcc, 4, v9\n\t"
    "s_cmp_eq_u64 vcc, -1\n\t"
    "s_cbranch_scc1 L_hfok_%=\n\t"
    "s_sleep 8\n\t"
    "s_branch L_hf_%=\n\t"
    "L_hfok_%=:\n\t"
    "L_hes_%=:\n\t"
    "s_and_b32 s37, s29, 12\n\t"
    "s_mul_i32 s37, s37, 7\n\t"
    "s_add_u32 s37, s37, s29\n\t"
    "s_lshl2_add_u32 s37, s37, 65536\n\t"
    "v_mov_b32 v7, s39\n\t"
    "buffer_store_dword v7, off, s[20:23], s37 sc1\n\t"
    "L_hend_%=:\n\t"
    "s_waitcnt vmcnt(0)\n\t"
    "L_end_%=:\n\t"
    "s_endpgm"
    :
    : "{s[0:1]}"(__builtin_amdgcn_kernarg_segment_ptr()), "{s2}"(__builtin_amdgcn_workgroup_id_x()), "{v0}"(__builtin_amdgcn_workitem_id_x()), "{s3}"((unsigned)(size_t)lds)
    : "v143", "a111", "s99", "vcc", "scc", "memory");
}

extern "C" void kernel_launch(void* const* d_in, const int* in_sizes, int n_in, void* d_out, int out_size,
                              void* d_ws, size_t ws_size, hipStream_t stream) {
  fused_mlp<<<256, 512, 0, stream>>>(d_in[0], d_in[1], d_in[2], d_out, d_ws);
}
```
